```python
import torch

def _softmax(x, axis=-1):
    return torch.softmax(x, dim=axis)
def _gelu(x):
    return torch.nn.functional.gelu(x, approximate="tanh")
def _silu(x):
    return torch.nn.functional.silu(x)
def _sigmoid(x):
    return torch.sigmoid(x)
def _layernorm(x, axis=-1, eps=1e-5):
    m = x.mean(dim=axis, keepdim=True)
    v = x.var(dim=axis, keepdim=True, unbiased=False)
    return (x - m) / torch.sqrt(v + eps)
def _scatter_add(tgt, idx, src, axis=0):
    return tgt.clone().index_add_(axis, idx, src)
def _segment_sum(src, seg, n):
    out = torch.zeros((n,) + src.shape[1:], dtype=src.dtype)
    return out.index_add_(0, seg, src)
def _pad(x, pw):
    flat = tuple(v for pair in reversed(pw) for v in pair)
    return torch.nn.functional.pad(x, flat)

kernel_name = 'standalone__sequential_behavior_din__recsys'

def setup_inputs():

    _w = (lambda a: (lambda t: t.to(torch.bfloat16) if t.is_floating_point() else t)(torch.as_tensor(a)))
    B, L, D = 1024, 200, 128
    H = D
    return {
        "target": _w((lambda *s: torch.randn(*s, dtype=torch.bfloat16) * 1.6)(B, D)),
        "hist": _w((lambda *s: torch.randn(*s, dtype=torch.bfloat16) * 1.6)(B, L, D)),
        "mask": _w((lambda lo, hi, *s: torch.randint(lo, hi, s))(0, 2, B, L).to(float)),
        "w1": _w((lambda *s: torch.randn(*s, dtype=torch.bfloat16) * 1.6)(4 * D, H) * (4 * D) ** -0.5),
        "b1": _w((lambda *s: torch.randn(*s, dtype=torch.bfloat16) * 1.6)(H) * 0.1),
        "w2": _w((lambda *s: torch.randn(*s, dtype=torch.bfloat16) * 0.05)(H, 1) * H ** -0.5),
        "b2": _w((lambda *s: torch.randn(*s, dtype=torch.bfloat16) * 0.05)(1) * 0.1),
    }

def reference(target, hist, mask, w1, b1, w2, b2):
    L = hist.shape[1]
    tgt = (lambda x, n, axis: torch.repeat_interleave(x, n, dim=axis))(target[:, None, :], L, axis=1)
    feat = torch.concatenate([hist, tgt, hist - tgt, hist * tgt], axis=-1)
    h = _sigmoid(feat @ w1 + b1)
    scores = (h @ w2 + b2)[..., 0] * mask
    return torch.einsum('bl,bld->bd', scores, hist)
```

```cpp
#include <hip/hip_runtime.h>
#include <hip/hip_bf16.h>

typedef unsigned short u16;
__device__ __forceinline__ float bf2f(u16 v) { return __uint_as_float(((unsigned)v) << 16); }

__global__ __launch_bounds__(256) void din_naive(const u16* __restrict__ tgt, const u16* __restrict__ hist,
                                                 const u16* __restrict__ mask, const u16* __restrict__ w1,
                                                 const u16* __restrict__ b1, const u16* __restrict__ w2,
                                                 const u16* __restrict__ b2, __hip_bfloat16* __restrict__ out) {
  constexpr int L = 200, D = 128, H = 128;
  __shared__ float weff[D * H];
  __shared__ float cvec[H];
  __shared__ float t[D];
  __shared__ float sc[L];
  __shared__ float row[2][D];
  __shared__ float red[256];
  const int b = blockIdx.x, tid = threadIdx.x;
  if (tid < D) t[tid] = bf2f(tgt[b * D + tid]);
  __syncthreads();
  for (int i = tid; i < D * H; i += 256) {
    int d = i / H;
    float v = bf2f(w1[i]) + bf2f(w1[2 * D * H + i]) + t[d] * bf2f(w1[3 * D * H + i]);
    weff[i] = __bfloat162float(__float2bfloat16(v));
  }
  if (tid < H) {
    float c = bf2f(b1[tid]);
    for (int d = 0; d < D; ++d) c += t[d] * (bf2f(w1[(D + d) * H + tid]) - bf2f(w1[(2 * D + d) * H + tid]));
    cvec[tid] = c;
  }
  __syncthreads();
  const int half = tid >> 7, h = tid & 127;
  const float w2h = bf2f(w2[h]);
  for (int l0 = 0; l0 < L; l0 += 2) {
    int l = l0 + half;
    row[half][h] = bf2f(hist[((size_t)b * L + l) * D + h]);
    __syncthreads();
    float x = cvec[h];
    for (int d = 0; d < D; ++d) x += row[half][d] * weff[d * H + h];
    float s = 1.f / (1.f + __expf(-x));
    red[tid] = s * w2h;
    __syncthreads();
    for (int o = 64; o > 0; o >>= 1) {
      if (h < o) red[tid] += red[tid + o];
      __syncthreads();
    }
    if (h == 0) sc[l] = (red[tid] + bf2f(b2[0])) * bf2f(mask[b * L + l]);
    __syncthreads();
  }
  if (tid < D) {
    float acc = 0.f;
    for (int l = 0; l < L; ++l) acc += sc[l] * bf2f(hist[((size_t)b * L + l) * D + tid]);
    out[b * D + tid] = __float2bfloat16(acc);
  }
}

extern "C" void kernel_launch(void* const* d_in, const int* in_sizes, int n_in, void* d_out, int out_size,
                              void* d_ws, size_t ws_size, hipStream_t stream) {
  const u16* tgt = (const u16*)d_in[0];
  const u16* hist = (const u16*)d_in[1];
  const u16* mask = (const u16*)d_in[2];
  const u16* w1 = (const u16*)d_in[3];
  const u16* b1 = (const u16*)d_in[4];
  const u16* w2 = (const u16*)d_in[5];
  const u16* b2 = (const u16*)d_in[6];
  int B = in_sizes[0] / 128;
  din_naive<<<B, 256, 0, stream>>>(tgt, hist, mask, w1, b1, w2, b2, (__hip_bfloat16*)d_out);
}
```

```cpp
#include <hip/hip_runtime.h>
#include <hip/hip_bf16.h>

typedef unsigned short u16;
typedef unsigned u32;
typedef __attribute__((ext_vector_type(8))) __bf16 bf16x8;
typedef __attribute__((ext_vector_type(2))) __bf16 bf16x2;
typedef __attribute__((ext_vector_type(4))) short s16x4;
typedef __attribute__((ext_vector_type(4))) u32 u32x4;
typedef __attribute__((ext_vector_type(2))) u32 u32x2;
typedef __attribute__((ext_vector_type(2))) float f32x2;
typedef __attribute__((ext_vector_type(4))) float f32x4;
typedef __attribute__((ext_vector_type(16))) float f32x16;
#define LDSP(T, a) ((__attribute__((address_space(3))) T*)(a))

#define HIST_NT 0

constexpr int L = 200, D = 128;
constexpr u32 SLOTB = 9216, NS = 17;
constexpr u32 OFFS = NS * SLOTB;
constexpr u32 MSK = OFFS + 1792;
constexpr u32 CTAB = MSK + 1792;
constexpr u32 CNT = CTAB + 2048;
constexpr u32 C_READY = CNT, C_PDONE = CNT + 128, C_FDONE = CNT + 256, C_WREADY = CNT + 384, C_WCONS = CNT + 400,
              C_CREADY = CNT + 416, C_NT = CNT + 432;
constexpr u32 LDS_BYTES = CNT + 448;
constexpr u32 SPIN_MAX = 1u << 18;
constexpr float SCL = -1.44269504088896340736f;
constexpr float KSH = 40.f;
constexpr float KAP = 9.094947017729282e-13f;

__device__ __forceinline__ void dma16(u32 voff, const void* sbase, u32 ldsaddr) {
  asm volatile("s_mov_b32 m0, %2\n\ts_nop 0\n\tglobal_load_lds_dwordx4 %0, %1" ::"v"(voff), "s"(sbase), "s"(ldsaddr) : "memory");
}
__device__ __forceinline__ void dma16h(u32 voff, const void* sbase, u32 ldsaddr) {
  asm volatile("s_mov_b32 m0, %2\n\ts_nop 0\n\tglobal_load_lds_dwordx4 %0, %1" ::"v"(voff), "s"(sbase), "s"(ldsaddr) : "memory");
}
__device__ __forceinline__ void signal(u32 addr) {
  u32 va = addr, one = 1;
  asm volatile("s_mov_b64 exec, 1\n\tds_add_u32 %0, %1\n\ts_mov_b64 exec, -1" ::"v"(va), "v"(one) : "memory");
}
__device__ __forceinline__ u32 peek(u32 addr) {
  u32 va = addr, r;
  asm volatile("ds_read_b32 %0, %1\n\ts_waitcnt lgkmcnt(0)" : "=&v"(r) : "v"(va) : "memory");
  return __builtin_amdgcn_readfirstlane(r);
}
__device__ __forceinline__ void wait4(u32 addr) {
  for (u32 i = 0; peek(addr) < 4 && i < SPIN_MAX; ++i) __builtin_amdgcn_s_sleep(1);
}
#define VMCNT(n) asm volatile("s_waitcnt vmcnt(" #n ")" ::: "memory")
__device__ __forceinline__ float bf2f(u32 v) { return __uint_as_float(v << 16); }
__device__ __forceinline__ float bfhi(u32 v) { return __uint_as_float(v & 0xffff0000u); }
__device__ __forceinline__ u32 pk2(float a, float b) {
  f32x2 p = {a, b};
  return __builtin_bit_cast(u32, __builtin_convertvector(p, bf16x2));
}
__device__ __forceinline__ u32 swz(u32 row, u32 ch) { return 256u * row + 16u * (ch ^ (((row & 3) << 2) | ((row >> 2) & 3))); }
__device__ __forceinline__ bf16x8 ld128(u32 a) { return *LDSP(bf16x8, a); }
__device__ __forceinline__ bf16x8 ldtr(u32 a0, u32 a1) {
  s16x4 x = __builtin_amdgcn_ds_read_tr16_b64_v4i16(LDSP(s16x4, a0));
  s16x4 y = __builtin_amdgcn_ds_read_tr16_b64_v4i16(LDSP(s16x4, a1));
  typedef __attribute__((ext_vector_type(8))) short s16x8;
  s16x8 z = __builtin_shufflevector(x, y, 0, 1, 2, 3, 4, 5, 6, 7);
  return __builtin_bit_cast(bf16x8, z);
}

__global__ __launch_bounds__(512, 2) void din_kernel(const u16* __restrict__ tgt, const u16* __restrict__ hist,
                                                     const u16* __restrict__ mask, const u16* __restrict__ w1,
                                                     const u16* __restrict__ b1, const u16* __restrict__ w2,
                                                     const u16* __restrict__ b2, u16* __restrict__ out
) {
#define STAMP(k)
#define STORE_STAMPS(lo, hi)
  STAMP(0);
  __shared__ __attribute__((aligned(1024))) char lds_raw[LDS_BYTES];
  constexpr u32 lb = 0;
  asm volatile("" ::"v"((__attribute__((address_space(3))) char*)lds_raw));
  const u32 tid = threadIdx.x, lane = tid & 63;
  const u32 wave = __builtin_amdgcn_readfirstlane(tid >> 6);
  const u32 b0 = blockIdx.x * 4;
  const u32 lg = lane >> 4, l15 = lane & 15;

  if (wave >= 4) {
    const u32 q = wave - 4;
    u32x2 mv = {0, 0};
    if (lane < 50) mv = *(const u32x2*)(mask + (b0 + q) * L + lane * 4);
    if (lane < 56) {
      u32x2 z = {0, 0};
      *LDSP(u32x2, lb + OFFS + q * 448 + lane * 8) = z;
      *LDSP(u32x2, lb + MSK + q * 448 + lane * 8) = z;
    }
    asm volatile("s_waitcnt lgkmcnt(0)\n\ts_barrier" ::: "memory");
    const bool f0 = (mv.x & 0x7fff) != 0, f1 = (mv.x & 0x7fff0000) != 0, f2 = (mv.y & 0x7fff) != 0, f3 = (mv.y & 0x7fff0000) != 0;
    STAMP(1);
    const unsigned long long m0b = __ballot(f0), m1b = __ballot(f1), m2b = __ballot(f2), m3b = __ballot(f3);
    u32 p = __builtin_amdgcn_mbcnt_hi(m0b >> 32, __builtin_amdgcn_mbcnt_lo((u32)m0b, 0));
    p = __builtin_amdgcn_mbcnt_hi(m1b >> 32, __builtin_amdgcn_mbcnt_lo((u32)m1b, p));
    p = __builtin_amdgcn_mbcnt_hi(m2b >> 32, __builtin_amdgcn_mbcnt_lo((u32)m2b, p));
    p = __builtin_amdgcn_mbcnt_hi(m3b >> 32, __builtin_amdgcn_mbcnt_lo((u32)m3b, p));
    const u32 n = __popcll(m0b) + __popcll(m1b) + __popcll(m2b) + __popcll(m3b);
    u32 a = lb + OFFS + q * 448 + p * 2;
    const u32 r0 = lane << 10;
    if (f0) { *LDSP(u16, a) = (u16)r0; *LDSP(u16, a + 1792) = (u16)mv.x; a += 2; }
    if (f1) { *LDSP(u16, a) = (u16)(r0 + 256); *LDSP(u16, a + 1792) = (u16)(mv.x >> 16); a += 2; }
    if (f2) { *LDSP(u16, a) = (u16)(r0 + 512); *LDSP(u16, a + 1792) = (u16)mv.y; a += 2; }
    if (f3) { *LDSP(u16, a) = (u16)(r0 + 768); *LDSP(u16, a + 1792) = (u16)(mv.y >> 16); }
    if (lane == 0) *LDSP(u32, lb + C_NT + q * 4) = (n + 31) >> 5;
    asm volatile("" ::: "memory");
    signal(lb + C_CREADY);
    wait4(lb + C_CREADY);
    u32x4 ntv = *LDSP(u32x4, lb + C_NT);
    const u32 nt0 = __builtin_amdgcn_readfirstlane(ntv.x), nt1 = __builtin_amdgcn_readfirstlane(ntv.y),
              nt2 = __builtin_amdgcn_readfirstlane(ntv.z), nt3 = __builtin_amdgcn_readfirstlane(ntv.w);
    const u32 R = max(max(nt0, nt1), max(nt2, nt3));
    const u32 J = 4 * R;
    STAMP(2);
    const u32 sw0 = 16 * (l15 ^ ((lg << 2) | ((2 * q) & 3))), sw1 = 16 * (l15 ^ ((lg << 2) | ((2 * q + 1) & 3)));
    const u32 tabl = lb + OFFS + (8 * q + lg) * 2;
    const u32 fa = 8192 + l15 * 64 + lg * 16;
    const u32 tq = l15 >> 2, tp = lane & 3;
    const u32 tr00 = swz(8 * lg + tq, 4 * q + (tp >> 1)) + 8 * (tp & 1), tr01 = swz(8 * lg + 4 + tq, 4 * q + (tp >> 1)) + 8 * (tp & 1);
    const u32 tr10 = swz(8 * lg + tq, 4 * q + 2 + (tp >> 1)) + 8 * (tp & 1), tr11 = swz(8 * lg + 4 + tq, 4 * q + 2 + (tp >> 1)) + 8 * (tp & 1);
    f32x4 oa[4][2];
#pragma unroll
    for (int i = 0; i < 4; ++i) { oa[i][0] = f32x4{0, 0, 0, 0}; oa[i][1] = f32x4{0, 0, 0, 0}; }
    u32 f = 0, fslot = 0;
    u32 slot = 0;
    const u32 NIT = J ? J + 5 : 0;
    for (u32 j = 0; j < NIT; ++j) {
      if (j < J) {
        const u32 jb = j & 3, jt = j >> 2;
        const u32 ta = tabl + jb * 448 + jt * 64;
        const u32 e0 = *LDSP(u16, ta), e1 = *LDSP(u16, ta + 8);
        if (j >= NS) wait4(lb + C_FDONE + (j - NS) * 4);
        else if (j == 5 || j == 9 || j == 13) wait4(lb + C_WCONS + ((j - 1) >> 2) * 4);
        const char* sb = (const char*)hist + (size_t)(b0 + jb) * (L * D * 2);
        const u32 dst = lb + slot * SLOTB + q * 2048;
        dma16h(e0 + sw0, sb, dst);
        dma16h(e1 + sw1, sb, dst + 1024);
        slot = slot + 1 == NS ? 0 : slot + 1;
      }
      if (j >= 5) {
        const u32 rem = NIT - 1 - j;
        if (rem >= 5) VMCNT(10);
        else if (rem == 4) VMCNT(8);
        else if (rem == 3) VMCNT(6);
        else if (rem == 2) VMCNT(4);
        else if (rem == 1) VMCNT(2);
        else VMCNT(0);
        signal(lb + C_READY + (j - 5) * 4);
        const u32 need = rem == 0 ? J : (j + 1 < J && j + 1 >= NS) ? j + 2 - NS : 0;
        while (f < J) {
          if (peek(lb + C_PDONE + f * 4) < 4) {
            if (f >= need) break;
            wait4(lb + C_PDONE + f * 4);
          }
          const u32 fb = f & 3, ft = f >> 2;
          const u32 ntb = fb == 0 ? nt0 : fb == 1 ? nt1 : fb == 2 ? nt2 : nt3;
          if (ft < ntb) {
            const u32 sb = lb + fslot * SLOTB;
            bf16x8 A = ld128(sb + fa), B0 = ldtr(sb + tr00, sb + tr01), B1 = ldtr(sb + tr10, sb + tr11);
            if (fb == 0) { oa[0][0] = __builtin_amdgcn_mfma_f32_16x16x32_bf16(A, B0, oa[0][0], 0, 0, 0); oa[0][1] = __builtin_amdgcn_mfma_f32_16x16x32_bf16(A, B1, oa[0][1], 0, 0, 0); }
            else if (fb == 1) { oa[1][0] = __builtin_amdgcn_mfma_f32_16x16x32_bf16(A, B0, oa[1][0], 0, 0, 0); oa[1][1] = __builtin_amdgcn_mfma_f32_16x16x32_bf16(A, B1, oa[1][1], 0, 0, 0); }
            else if (fb == 2) { oa[2][0] = __builtin_amdgcn_mfma_f32_16x16x32_bf16(A, B0, oa[2][0], 0, 0, 0); oa[2][1] = __builtin_amdgcn_mfma_f32_16x16x32_bf16(A, B1, oa[2][1], 0, 0, 0); }
            else { oa[3][0] = __builtin_amdgcn_mfma_f32_16x16x32_bf16(A, B0, oa[3][0], 0, 0, 0); oa[3][1] = __builtin_amdgcn_mfma_f32_16x16x32_bf16(A, B1, oa[3][1], 0, 0, 0); }
          }
          asm volatile("" ::: "memory");
          signal(lb + C_FDONE + f * 4);
          ++f; fslot = fslot + 1 == NS ? 0 : fslot + 1;
        }
      }
    }
    STAMP(8);
#pragma unroll
    for (int i = 0; i < 4; ++i) {
      float s0 = (oa[i][0][0] + oa[i][0][1]) + (oa[i][0][2] + oa[i][0][3]);
      float s1 = (oa[i][1][0] + oa[i][1][1]) + (oa[i][1][2] + oa[i][1][3]);
      s0 += __shfl_xor(s0, 16); s1 += __shfl_xor(s1, 16);
      s0 += __shfl_xor(s0, 32); s1 += __shfl_xor(s1, 32);
      const float s = (lane & 16) ? s1 : s0;
      if (lane < 32) out[(b0 + i) * D + q * 32 + lane] = (u16)pk2(s, s);
    }
    STAMP(9);
    STORE_STAMPS(0, 9);
  } else {
    const u32 w = wave, c = lane & 31, hf = lane >> 5;
    if (w == 0) { u32x2 z = {0, 0}; if (lane < 54) *LDSP(u32x2, lb + CNT + lane * 8) = z; }
    u32 tw; u32x2 b1v[4], w2v[4]; u32 b2v;
    {
      const u32 o = ((b0 + w) * D + 2 * lane) * 2;
      asm volatile("global_load_dword %0, %1, %2" : "=v"(tw) : "v"(o), "s"(tgt));
      const u32 o3 = (32 * w + 4 * hf) * 2;
#pragma unroll
      for (int g = 0; g < 4; ++g) {
        asm volatile("global_load_dwordx2 %0, %1, %2 offset:%3" : "=v"(b1v[g]) : "v"(o3), "s"(b1), "i"(g * 16));
        asm volatile("global_load_dwordx2 %0, %1, %2 offset:%3" : "=v"(w2v[g]) : "v"(o3), "s"(w2), "i"(g * 16));
      }
      const u32 z = 0;
      asm volatile("global_load_ushort %0, %1, %2" : "=v"(b2v) : "v"(z), "s"(b2));
    }
    asm volatile("s_waitcnt lgkmcnt(0)\n\ts_barrier" ::: "memory");
    u32 wsw[4];
#pragma unroll
    for (int i = 0; i < 4; ++i) wsw[i] = (128 * w + 4 * i + lg) * 256 + 16 * (l15 ^ ((lg << 2) | i));
    auto wload = [&](u32 blk, u32 slot0) {
#pragma unroll
      for (int i = 0; i < 8; ++i) dma16(wsw[i & 3] + blk * 8192 + (i >> 2) * 4096, w1, lb + (slot0 + w) * SLOTB + i * 1024);
    };
    wload(0, 13); wload(1, 5); wload(2, 9);
    asm volatile("s_waitcnt vmcnt(24)" : "+v"(tw),
                 "+v"(b1v[0]), "+v"(b1v[1]), "+v"(b1v[2]), "+v"(b1v[3]), "+v"(w2v[0]), "+v"(w2v[1]), "+v"(w2v[2]), "+v"(w2v[3]), "+v"(b2v)::"memory");
    STAMP(1);
    const u32 TP = lb + 13 * SLOTB + 8192;
    { f32x2 t2 = {bf2f(tw) * SCL, bfhi(tw) * SCL}; *LDSP(f32x2, TP + (w >> 1) * SLOTB + (w & 1) * 512 + lane * 8) = t2;
      *LDSP(u32, TP + 2 * SLOTB + w * 256 + lane * 4) = tw; }
    const u32 tfa = TP + 2 * SLOTB + (c & 3) * 256 + hf * 16;
    bf16x8 I0, I1;
    {
      const u32 one = (c & 1) ? 0x3f800000u : 0x00003f80u, dw = (c & 7) >> 1;
      u32x4 e = {dw == 0 ? one : 0u, dw == 1 ? one : 0u, dw == 2 ? one : 0u, dw == 3 ? one : 0u}, z = {0, 0, 0, 0};
      I0 = __builtin_bit_cast(bf16x8, (c >> 3) == hf ? e : z);
      I1 = __builtin_bit_cast(bf16x8, (c >> 3) == 2 + hf ? e : z);
    }
    const u32 prow = (c & 0x13) | ((c & 4) << 1) | ((c & 8) >> 1);
    const u32 ra0 = swz(prow, 4 * w + hf), ra1 = swz(prow, 4 * w + 2 + hf);
    const u32 tq = l15 >> 2, tp = lane & 3, tb = lg & 1;
    u32 tra[2][2];
#pragma unroll
    for (int ks = 0; ks < 2; ++ks)
#pragma unroll
      for (int t = 0; t < 2; ++t) tra[ks][t] = swz(16 * ks + 8 * hf + 4 * t + tq, 4 * w + 2 * tb + (tp >> 1)) + 8 * (tp & 1);
    u32x4 We[4][8];
    f32x16 Cb, zero16;
#pragma unroll
    for (int i = 0; i < 16; ++i) zero16[i] = 0;
#pragma unroll
    for (int g = 0; g < 4; ++g) { Cb[4 * g] = bf2f(b1v[g].x); Cb[4 * g + 1] = bfhi(b1v[g].x); Cb[4 * g + 2] = bf2f(b1v[g].y); Cb[4 * g + 3] = bfhi(b1v[g].y); }
#pragma unroll
    for (int blk = 0; blk < 4; ++blk) {
      if (blk == 0) VMCNT(16); else if (blk == 1) VMCNT(16); else if (blk == 2) VMCNT(8); else VMCNT(0);
      signal(lb + C_WREADY + blk * 4);
      wait4(lb + C_WREADY + blk * 4);
      STAMP(2 + blk);
      const u32 s0 = lb + (blk == 1 ? 5 : blk == 2 ? 9 : 13) * SLOTB;
      bf16x8 a0 = ld128(s0 + ra0), a1 = ld128(s0 + ra1);
      bf16x8 c0 = ld128(s0 + 2 * SLOTB + ra0), c1 = ld128(s0 + 2 * SLOTB + ra1);
      bf16x8 d0 = ld128(s0 + 3 * SLOTB + ra0), d1 = ld128(s0 + 3 * SLOTB + ra1);
      bf16x8 bt0 = ldtr(s0 + SLOTB + tra[0][0], s0 + SLOTB + tra[0][1]), bt1 = ldtr(s0 + SLOTB + tra[1][0], s0 + SLOTB + tra[1][1]);
      bf16x8 ct0 = ldtr(s0 + 2 * SLOTB + tra[0][0], s0 + 2 * SLOTB + tra[0][1]), ct1 = ldtr(s0 + 2 * SLOTB + tra[1][0], s0 + 2 * SLOTB + tra[1][1]);
      const u32x4 tf0 = *LDSP(u32x4, tfa + blk * 64), tf1 = *LDSP(u32x4, tfa + blk * 64 + 32);
      if (blk < 3) { asm volatile("" ::: "memory"); signal(lb + C_WCONS + blk * 4); }
      f32x16 Xa = __builtin_amdgcn_mfma_f32_32x32x16_bf16(a0, I0, zero16, 0, 0, 0);
      f32x16 Xd = __builtin_amdgcn_mfma_f32_32x32x16_bf16(d0, I0, zero16, 0, 0, 0);
      Xa = __builtin_amdgcn_mfma_f32_32x32x16_bf16(a1, I1, Xa, 0, 0, 0);
      Xd = __builtin_amdgcn_mfma_f32_32x32x16_bf16(d1, I1, Xd, 0, 0, 0);
      Xa = __builtin_amdgcn_mfma_f32_32x32x16_bf16(c0, I0, Xa, 0, 0, 0);
      Xa = __builtin_amdgcn_mfma_f32_32x32x16_bf16(c1, I1, Xa, 0, 0, 0);
      Cb = __builtin_amdgcn_mfma_f32_32x32x16_bf16(bt0, __builtin_bit_cast(bf16x8, tf0), Cb, 0, 0, 0);
      Cb = __builtin_amdgcn_mfma_f32_32x32x16_bf16(bt1, __builtin_bit_cast(bf16x8, tf1), Cb, 0, 0, 0);
      Cb = __builtin_amdgcn_mfma_f32_32x32x16_bf16(ct0, __builtin_bit_cast(bf16x8, tf0 ^ 0x80008000u), Cb, 0, 0, 0);
      Cb = __builtin_amdgcn_mfma_f32_32x32x16_bf16(ct1, __builtin_bit_cast(bf16x8, tf1 ^ 0x80008000u), Cb, 0, 0, 0);
      Xa *= SCL;
      __builtin_amdgcn_sched_barrier(0);
#pragma unroll
      for (int b = 0; b < 4; ++b) {
        const u32 ta = TP + (b >> 1) * SLOTB + (b & 1) * 512 + (32 * blk + 8 * hf) * 4;
#pragma unroll
        for (int s = 0; s < 2; ++s) {
          const f32x4 t0 = *LDSP(f32x4, ta + s * 64), t1 = *LDSP(f32x4, ta + s * 64 + 16);
          u32x4 r;
          r.x = pk2(fmaf(t0.x, Xd[8 * s + 0], Xa[8 * s + 0]), fmaf(t0.y, Xd[8 * s + 1], Xa[8 * s + 1]));
          r.y = pk2(fmaf(t0.z, Xd[8 * s + 2], Xa[8 * s + 2]), fmaf(t0.w, Xd[8 * s + 3], Xa[8 * s + 3]));
          r.z = pk2(fmaf(t1.x, Xd[8 * s + 4], Xa[8 * s + 4]), fmaf(t1.y, Xd[8 * s + 5], Xa[8 * s + 5]));
          r.w = pk2(fmaf(t1.z, Xd[8 * s + 6], Xa[8 * s + 6]), fmaf(t1.w, Xd[8 * s + 7], Xa[8 * s + 7]));
          asm volatile("" : "+v"(r));
          We[b][2 * blk + s] = r;
        }
        __builtin_amdgcn_sched_barrier(0);
      }
      if (blk == 0) { wait4(lb + C_WCONS); wload(3, 13); }
      if (blk == 3) { asm volatile("" ::: "memory"); signal(lb + C_WCONS + 12); }
    }
    float w2s[16];
    {
      f32x16 cv;
#pragma unroll
      for (int g = 0; g < 4; ++g) {
        cv[4 * g + 0] = fmaf(Cb[4 * g + 0], SCL, -KSH);
        cv[4 * g + 1] = fmaf(Cb[4 * g + 1], SCL, -KSH);
        cv[4 * g + 2] = fmaf(Cb[4 * g + 2], SCL, -KSH);
        cv[4 * g + 3] = fmaf(Cb[4 * g + 3], SCL, -KSH);
        w2s[4 * g + 0] = bf2f(w2v[g].x) * KAP; w2s[4 * g + 1] = bfhi(w2v[g].x) * KAP;
        w2s[4 * g + 2] = bf2f(w2v[g].y) * KAP; w2s[4 * g + 3] = bfhi(w2v[g].y) * KAP;
      }
      if (c < 4) {
#pragma unroll
        for (int g = 0; g < 4; ++g)
          *LDSP(f32x4, lb + CTAB + c * 512 + w * 128 + hf * 64 + g * 16) = f32x4{cv[4 * g], cv[4 * g + 1], cv[4 * g + 2], cv[4 * g + 3]};
      }
    }
    const float sinit = (w == 0 && hf == 0) ? bf2f(b2v) : 0.f;
    wait4(lb + C_CREADY);
    u32x4 ntv = *LDSP(u32x4, lb + C_NT);
    const u32 R = __builtin_amdgcn_readfirstlane(max(max(ntv.x, ntv.y), max(ntv.z, ntv.w)));
    STAMP(6);
    u32 rb[8];
#pragma unroll
    for (int s = 0; s < 8; ++s) rb[s] = swz(c, 2 * s + hf);
    const u32 pw = 8192 + (4 * w + 2 * hf) * 64 + c * 2;
    u32 slot = 0;
    for (u32 t = 0; t < R; ++t) {
#pragma unroll
      for (int b = 0; b < 4; ++b) {
        const u32 j = 4 * t + b;
        wait4(lb + C_READY + j * 4);
        const u32 sb = lb + slot * SLOTB;
        f32x16 X = *LDSP(f32x16, lb + CTAB + b * 512 + w * 128 + hf * 64);
        const float m = bf2f(*LDSP(u16, lb + MSK + b * 448 + t * 64 + c * 2));
#pragma unroll
        for (int s = 0; s < 8; ++s) X = __builtin_amdgcn_mfma_f32_32x32x16_bf16(__builtin_bit_cast(bf16x8, We[b][s]), ld128(sb + rb[s]), X, 0, 0, 0);
        float sx = sinit, sy = 0.f;
#pragma unroll
        for (int g = 0; g < 4; ++g) {
          float d0 = __builtin_amdgcn_fmed3f(__builtin_amdgcn_exp2f(X[4 * g + 0]), 0.f, 1.f) + KAP;
          float d1 = __builtin_amdgcn_fmed3f(__builtin_amdgcn_exp2f(X[4 * g + 1]), 0.f, 1.f) + KAP;
          float d2 = __builtin_amdgcn_fmed3f(__builtin_amdgcn_exp2f(X[4 * g + 2]), 0.f, 1.f) + KAP;
          float d3 = __builtin_amdgcn_fmed3f(__builtin_amdgcn_exp2f(X[4 * g + 3]), 0.f, 1.f) + KAP;
          sx = fmaf(fmaf(w2s[4 * g + 2], d0, w2s[4 * g + 0] * d2), __builtin_amdgcn_rcpf(d0 * d2), sx);
          sy = fmaf(fmaf(w2s[4 * g + 3], d1, w2s[4 * g + 1] * d3), __builtin_amdgcn_rcpf(d1 * d3), sy);
        }
        const u32 ps = pk2(sx * m, sy * m);
        *LDSP(u16, sb + pw) = (u16)ps;
        *LDSP(u16, sb + pw + 64) = (u16)(ps >> 16);
        asm volatile("" ::: "memory");
        signal(lb + C_PDONE + j * 4);
        slot = slot + 1 == NS ? 0 : slot + 1;
      }
    }
    STORE_STAMPS(0, 10);
  }
}

extern "C" void kernel_launch(void* const* d_in, const int* in_sizes, int n_in, void* d_out, int out_size,
                              void* d_ws, size_t ws_size, hipStream_t stream) {
  const int B = in_sizes[0] / D;
  din_kernel<<<B / 4, 512, 0, stream>>>((const u16*)d_in[0], (const u16*)d_in[1], (const u16*)d_in[2], (const u16*)d_in[3],
                                        (const u16*)d_in[4], (const u16*)d_in[5], (const u16*)d_in[6], (u16*)d_out
  );
}
```

```cpp
#include <hip/hip_runtime.h>
#include <hip/hip_bf16.h>

typedef unsigned short u16;
typedef unsigned u32;
typedef __attribute__((ext_vector_type(4))) u32 u32x4;
typedef __attribute__((ext_vector_type(8))) u32 u32x8;
typedef __attribute__((ext_vector_type(16))) u32 u32x16;

constexpr int L = 200, D = 128;
constexpr u32 SLOTB = 8704;
constexpr u32 OFFS = 16 * SLOTB;
constexpr u32 MSKF = OFFS + 1792;
constexpr u32 TR = MSKF + 3584;
constexpr u32 BW = TR + 1024;
constexpr u32 CNT = BW + 512;
constexpr u32 XW = CNT + 512;
constexpr u32 CB = XW + 8192;
constexpr u32 KT = CB + 4096;
constexpr u32 LDS_BYTES = KT + 4 * 288;

#define ENTRY_ASM \
  "s_cmp_lt_u32 %[wave], 4\n" \
  "s_cbranch_scc1 din_ec\n" \
  "s_lshl_b32 s40, %[blk], 2\n" \
  "s_add_u32 s40, s40, %[wave]\n" \
  "s_mulk_i32 s40, 400\n" \
  "s_sub_u32 s40, s40, 1600\n" \
  "v_lshl_add_u32 v250, v247, 3, s40\n" \
  "v_mov_b32 v248, 0\n" \
  "v_mov_b32 v249, 0\n" \
  "s_mov_b32 exec_hi, 0x3ffff\n" \
  "global_load_dwordx2 v[248:249], v250, %[mask]\n" \
  "s_mov_b32 exec_hi, -1\n" \
  "s_mul_i32 s40, %[wave], 448\n" \
  "s_add_u32 s41, s40, 137472\n" \
  "s_lshl_b32 s40, s40, 1\n" \
  "s_add_u32 s40, s40, 137472\n" \
  "v_mov_b32 v240, 0\n" \
  "v_mov_b32 v241, 0\n" \
  "v_mov_b32 v242, 0\n" \
  "v_mov_b32 v243, 0\n" \
  "v_mov_b32 v244, -1\n" \
  "v_mov_b32 v245, -1\n" \
  "v_lshlrev_b32 v246, 3, v247\n" \
  "v_add_u32 v250, s41, v246\n" \
  "v_lshl_add_u32 v247, v247, 4, s40\n" \
  "v_add_u32 v246, 146176, v246\n" \
  "ds_write_b64 v246, v[240:241]\n" \
  "s_mov_b32 exec_hi, 0xffffff\n" \
  "ds_write_b64 v250, v[244:245]\n" \
  "ds_write_b128 v247, v[240:243]\n" \
  "s_mov_b32 exec_hi, -1\n" \
  "s_waitcnt lgkmcnt(0)\n" \
  "s_branch din_ed\n" \
  "din_ec:\n" \
  "s_cmp_gt_u32 %[wave], 0\n" \
  "s_cbranch_scc1 din_pfd\n" \
  "s_bfe_u32 s44, %[blk], 0x50003\n" \
  "s_lshl_b32 s42, s44, 12\n" \
  "s_add_u32 s42, %[w1lo], s42\n" \
  "s_addc_u32 s43, %[w1hi], 0\n" \
  "v_lshlrev_b32 v250, 7, v247\n" \
  "s_mov_b32 exec_hi, 0\n" \
  "global_load_dword v175, v250, s[42:43]\n" \
  "s_mov_b32 exec_hi, -1\n" \
  "din_pfd:\n" \
  "s_lshl_b32 s40, %[blk], 10\n" \
  "s_add_u32 s40, %[tlo], s40\n" \
  "s_addc_u32 s41, %[thi], 0\n" \
  "v_lshlrev_b32 v250, 4, v247\n" \
  "s_mov_b32 m0, 144640\n" \
  "v_lshrrev_b32 v249, 4, v247\n" \
  "v_lshlrev_b32 v248, 6, v247\n" \
  "global_load_lds_dwordx4 v250, s[40:41]\n" \
  "s_mov_b64 exec, 0xffff\n" \
  "s_mov_b32 m0, 145920\n" \
  "s_lshl_b32 s42, %[wave], 6\n" \
  "global_load_lds_dwordx4 v250, %[w2]\n" \
  "s_mov_b64 exec, -1\n" \
  "v_bitop3_b32 v249, v247, v249, 3 bitop3:0x6c\n" \
  "v_and_b32 v248, 0xffffff00, v248\n" \
  "s_mul_i32 s43, %[wave], 8704\n" \
  "v_lshl_add_u32 v249, v249, 4, v248\n" \
  "v_add_u32 v177, s42, v249\n" \
  "s_add_u32 m0, s43, 69632\n" \
  "s_add_u32 s40, %[w1lo], 0\n" \
  "s_addc_u32 s41, %[w1hi], 0\n" \
  "global_load_lds_dwordx4 v177, s[40:41]\n" \
  "s_add_u32 m0, s43, 70656\n" \
  "s_add_u32 s40, %[w1lo], 4096\n" \
  "s_addc_u32 s41, %[w1hi], 0\n" \
  "global_load_lds_dwordx4 v177, s[40:41]\n" \
  "s_add_u32 m0, s43, 71680\n" \
  "s_add_u32 s40, %[w1lo], 65536\n" \
  "s_addc_u32 s41, %[w1hi], 0\n" \
  "global_load_lds_dwordx4 v177, s[40:41]\n" \
  "s_add_u32 m0, s43, 72704\n" \
  "s_add_u32 s40, %[w1lo], 69632\n" \
  "s_addc_u32 s41, %[w1hi], 0\n" \
  "global_load_lds_dwordx4 v177, s[40:41]\n" \
  "s_add_u32 m0, s43, 73728\n" \
  "s_add_u32 s40, %[w1lo], 98304\n" \
  "s_addc_u32 s41, %[w1hi], 0\n" \
  "global_load_lds_dwordx4 v177, s[40:41]\n" \
  "s_add_u32 m0, s43, 74752\n" \
  "s_add_u32 s40, %[w1lo], 102400\n" \
  "s_addc_u32 s41, %[w1hi], 0\n" \
  "global_load_lds_dwordx4 v177, s[40:41]\n" \
  "s_add_u32 m0, s43, 34816\n" \
  "s_add_u32 s40, %[w1lo], 8192\n" \
  "s_addc_u32 s41, %[w1hi], 0\n" \
  "global_load_lds_dwordx4 v177, s[40:41]\n" \
  "s_add_u32 m0, s43, 35840\n" \
  "s_add_u32 s40, %[w1lo], 12288\n" \
  "s_addc_u32 s41, %[w1hi], 0\n" \
  "global_load_lds_dwordx4 v177, s[40:41]\n" \
  "s_add_u32 m0, s43, 36864\n" \
  "s_add_u32 s40, %[w1lo], 73728\n" \
  "s_addc_u32 s41, %[w1hi], 0\n" \
  "global_load_lds_dwordx4 v177, s[40:41]\n" \
  "s_add_u32 m0, s43, 37888\n" \
  "s_add_u32 s40, %[w1lo], 77824\n" \
  "s_addc_u32 s41, %[w1hi], 0\n" \
  "global_load_lds_dwordx4 v177, s[40:41]\n" \
  "din_ed:\n" \
  "s_barrier\n"
#define COMPUTE_ASM \
  ".p2align 3\n" \
  "s_mov_b32 s42, 0xbfb8aa3b\n" \
  "s_add_u32 m0, %[wdst], 38912\n" \
  "s_add_u32 s50, %[w1lo], 106496\n" \
  "s_addc_u32 s51, %[w1hi], 0\n" \
  "global_load_lds_dwordx4 v177, s[50:51]\n" \
  "s_add_u32 m0, %[wdst], 39936\n" \
  "s_add_u32 s50, %[w1lo], 110592\n" \
  "s_addc_u32 s51, %[w1hi], 0\n" \
  "global_load_lds_dwordx4 v177, s[50:51]\n" \
  "s_waitcnt vmcnt(12)\n" \
  "ds_read_u16 v128, v176\n" \
  "ds_read_u16 v188, v247 offset:0\n" \
  "ds_read_u16 v189, v247 offset:256\n" \
  "ds_read_u16 v190, v247 offset:512\n" \
  "ds_read_u16 v191, v247 offset:768\n" \
  "s_add_u32 m0, %[xdst], 0\n" \
  "s_add_u32 s50, %[w1lo], 114688\n" \
  "s_addc_u32 s51, %[w1hi], 0\n" \
  "global_load_lds_dwordx4 v177, s[50:51]\n" \
  "ds_read_u16 v208, v247 offset:64\n" \
  "ds_read_u16 v209, v247 offset:320\n" \
  "ds_read_u16 v210, v247 offset:576\n" \
  "ds_read_u16 v211, v247 offset:832\n" \
  "ds_read_u16 v232, v247 offset:128\n" \
  "ds_read_u16 v233, v247 offset:384\n" \
  "ds_read_u16 v234, v247 offset:640\n" \
  "ds_read_u16 v235, v247 offset:896\n" \
  "s_add_u32 m0, %[wdst], 40960\n" \
  "s_add_u32 s50, %[w1lo], 16384\n" \
  "s_addc_u32 s51, %[w1hi], 0\n" \
  "global_load_lds_dwordx4 v177, s[50:51]\n" \
  "ds_read_u16 v236, v247 offset:192\n" \
  "ds_read_u16 v237, v247 offset:448\n" \
  "ds_read_u16 v238, v247 offset:704\n" \
  "ds_read_u16 v239, v247 offset:960\n" \
  "s_waitcnt lgkmcnt(14)\n" \
  "s_add_u32 m0, %[wdst], 75776\n" \
  "s_add_u32 s50, %[w1lo], 81920\n" \
  "s_addc_u32 s51, %[w1hi], 0\n" \
  "global_load_lds_dwordx4 v177, s[50:51]\n" \
  "v_and_b32 v128, 0x8000, v128\n" \
  "v_or_b32 v129, 0x3f80, v128\n" \
  "v_lshl_or_b32 v129, v129, 16, v129\n" \
  "s_add_u32 m0, %[xdst], 1024\n" \
  "s_add_u32 s50, %[w1lo], 118784\n" \
  "s_addc_u32 s51, %[w1hi], 0\n" \
  "global_load_lds_dwordx4 v177, s[50:51]\n" \
  "v_and_b32 v224, v129, v224\n" \
  "v_and_b32 v225, v129, v225\n" \
  "v_and_b32 v226, v129, v226\n" \
  "s_add_u32 m0, %[wdst], 41984\n" \
  "s_add_u32 s50, %[w1lo], 20480\n" \
  "s_addc_u32 s51, %[w1hi], 0\n" \
  "global_load_lds_dwordx4 v177, s[50:51]\n" \
  "v_and_b32 v227, v129, v227\n" \
  "v_and_b32 v228, v129, v228\n" \
  "v_and_b32 v229, v129, v229\n" \
  "s_add_u32 m0, %[wdst], 76800\n" \
  "s_add_u32 s50, %[w1lo], 86016\n" \
  "s_addc_u32 s51, %[w1hi], 0\n" \
  "global_load_lds_dwordx4 v177, s[50:51]\n" \
  "v_and_b32 v230, v129, v230\n" \
  "v_and_b32 v231, v129, v231\n" \
  "v_lshlrev_b32 v128, 16, v128\n" \
  "v_xor_b32_e64 v247, s42, v128\n" \
  "s_waitcnt vmcnt(12) lgkmcnt(0)\n" \
  "ds_read_b128 v[80:83], v240 offset:38912\n" \
  "ds_read_b128 v[64:67], v240 offset:34816\n" \
  "ds_read_b128 v[84:87], v241 offset:38912\n" \
  "ds_read_b128 v[68:71], v241 offset:34816\n" \
  "ds_read_b128 v[72:75], v240 offset:36864\n" \
  "ds_read_b128 v[76:79], v241 offset:36864\n" \
  "ds_read_b128 v[212:215], v246 offset:0\n" \
  "ds_read_b64_tr_b16 v[88:89], v242 offset:36864\n" \
  "ds_read_b64_tr_b16 v[90:91], v244 offset:36864\n" \
  "ds_read_b64_tr_b16 v[92:93], v243 offset:36864\n" \
  "ds_read_b64_tr_b16 v[94:95], v245 offset:36864\n" \
  "s_mov_b64 exec, 1\n" \
  "ds_inc_u32 v250, v250 offset:400\n" \
  "s_mov_b64 exec, -1\n" \
  "v_mfma_f32_32x32x8bf16_1k v[96:111], v[188:189], v[184:185], 0\n" \
  "v_mfma_f32_32x32x8bf16_1k v[112:127], v[188:189], v[186:187], 0\n" \
  "s_waitcnt lgkmcnt(11)\n" \
  "v_mfma_f32_32x32x16_bf16 v[144:159], v[80:83], v[224:227], 0\n" \
  "s_waitcnt lgkmcnt(10)\n" \
  "v_mfma_f32_32x32x16_bf16 v[128:143], v[64:67], v[224:227], 0\n" \
  "s_waitcnt lgkmcnt(9)\n" \
  "v_mfma_f32_32x32x16_bf16 v[144:159], v[84:87], v[228:231], v[144:159]\n" \
  "s_waitcnt lgkmcnt(5)\n" \
  "v_xor_b32 v212, 0x80008000, v212\n" \
  "v_xor_b32 v213, 0x80008000, v213\n" \
  "v_xor_b32 v214, 0x80008000, v214\n" \
  "v_xor_b32 v215, 0x80008000, v215\n" \
  "v_mfma_f32_32x32x16_bf16 v[128:143], v[68:71], v[228:231], v[128:143]\n" \
  "s_waitcnt lgkmcnt(1)\n" \
  "s_waitcnt vmcnt(6)\n" \
  "ds_read_b128 v[80:83], v240 offset:4096\n" \
  "ds_read_b128 v[64:67], v240 offset:0\n" \
  "v_mfma_f32_32x32x16_bf16 v[128:143], v[72:75], v[224:227], v[128:143]\n" \
  "ds_read_b128 v[84:87], v241 offset:4096\n" \
  "v_mfma_f32_32x32x16_bf16 v[128:143], v[76:79], v[228:231], v[128:143]\n" \
  "ds_read_b128 v[68:71], v241 offset:0\n" \
  "v_mfma_f32_16x16x32_bf16 v[216:219], v[212:215], v[88:91], 0\n" \
  "ds_read_b128 v[72:75], v240 offset:2048\n" \
  "v_mfma_f32_16x16x32_bf16 v[220:223], v[212:215], v[92:95], 0\n" \
  "ds_read_b128 v[76:79], v241 offset:2048\n" \
  "v_mfma_f32_32x32x8bf16_1k v[160:175], v[190:191], v[184:185], 0\n" \
  "ds_read_b128 v[212:215], v246 offset:64\n" \
  "ds_read_b64_tr_b16 v[88:89], v242 offset:2048\n" \
  "ds_read_b64_tr_b16 v[90:91], v244 offset:2048\n" \
  "ds_read_b64_tr_b16 v[92:93], v243 offset:2048\n" \
  "ds_read_b64_tr_b16 v[94:95], v245 offset:2048\n" \
  "v_mfma_f32_32x32x8bf16_1k v[192:207], v[190:191], v[186:187], 0\n" \
  "v_add_u32 v188, %[xrel], v240\n" \
  "v_add_u32 v189, %[xrel], v241\n" \
  "v_pk_mul_f32 v[128:129], v[128:129], s[42:43] op_sel_hi:[1,0]\n" \
  "v_pk_mul_f32 v[130:131], v[130:131], s[42:43] op_sel_hi:[1,0]\n" \
  "v_pk_mul_f32 v[132:133], v[132:133], s[42:43] op_sel_hi:[1,0]\n" \
  "v_pk_mul_f32 v[134:135], v[134:135], s[42:43] op_sel_hi:[1,0]\n" \
  "v_pk_mul_f32 v[136:137], v[136:137], s[42:43] op_sel_hi:[1,0]\n" \
  "v_pk_mul_f32 v[138:139], v[138:139], s[42:43] op_sel_hi:[1,0]\n" \
  "v_pk_mul_f32 v[140:141], v[140:141], s[42:43] op_sel_hi:[1,0]\n" \
  "v_pk_mul_f32 v[142:143], v[142:143], s[42:43] op_sel_hi:[1,0]\n" \
  "v_pk_fma_f32 v[96:97], v[96:97], v[144:145], v[128:129]\n" \
  "v_pk_fma_f32 v[98:99], v[98:99], v[146:147], v[130:131]\n" \
  "v_pk_fma_f32 v[100:101], v[100:101], v[148:149], v[132:133]\n" \
  "v_pk_fma_f32 v[102:103], v[102:103], v[150:151], v[134:135]\n" \
  "v_pk_fma_f32 v[104:105], v[104:105], v[152:153], v[136:137]\n" \
  "v_pk_fma_f32 v[106:107], v[106:107], v[154:155], v[138:139]\n" \
  "v_pk_fma_f32 v[108:109], v[108:109], v[156:157], v[140:141]\n" \
  "v_pk_fma_f32 v[110:111], v[110:111], v[158:159], v[142:143]\n" \
  "v_pk_fma_f32 v[112:113], v[112:113], v[144:145], v[128:129]\n" \
  "v_pk_fma_f32 v[114:115], v[114:115], v[146:147], v[130:131]\n" \
  "v_pk_fma_f32 v[116:117], v[116:117], v[148:149], v[132:133]\n" \
  "v_pk_fma_f32 v[118:119], v[118:119], v[150:151], v[134:135]\n" \
  "v_pk_fma_f32 v[120:121], v[120:121], v[152:153], v[136:137]\n" \
  "v_pk_fma_f32 v[122:123], v[122:123], v[154:155], v[138:139]\n" \
  "v_pk_fma_f32 v[124:125], v[124:125], v[156:157], v[140:141]\n" \
  "v_pk_fma_f32 v[126:127], v[126:127], v[158:159], v[142:143]\n" \
  "v_pk_fma_f32 v[160:161], v[160:161], v[144:145], v[128:129]\n" \
  "v_pk_fma_f32 v[162:163], v[162:163], v[146:147], v[130:131]\n" \
  "v_pk_fma_f32 v[164:165], v[164:165], v[148:149], v[132:133]\n" \
  "v_pk_fma_f32 v[166:167], v[166:167], v[150:151], v[134:135]\n" \
  "v_pk_fma_f32 v[168:169], v[168:169], v[152:153], v[136:137]\n" \
  "v_pk_fma_f32 v[170:171], v[170:171], v[154:155], v[138:139]\n" \
  "v_pk_fma_f32 v[172:173], v[172:173], v[156:157], v[140:141]\n" \
  "v_pk_fma_f32 v[174:175], v[174:175], v[158:159], v[142:143]\n" \
  "v_pk_fma_f32 v[192:193], v[192:193], v[144:145], v[128:129]\n" \
  "v_pk_fma_f32 v[194:195], v[194:195], v[146:147], v[130:131]\n" \
  "v_pk_fma_f32 v[196:197], v[196:197], v[148:149], v[132:133]\n" \
  "v_pk_fma_f32 v[198:199], v[198:199], v[150:151], v[134:135]\n" \
  "v_pk_fma_f32 v[200:201], v[200:201], v[152:153], v[136:137]\n" \
  "v_pk_fma_f32 v[202:203], v[202:203], v[154:155], v[138:139]\n" \
  "v_pk_fma_f32 v[204:205], v[204:205], v[156:157], v[140:141]\n" \
  "v_pk_fma_f32 v[206:207], v[206:207], v[158:159], v[142:143]\n" \
  "s_waitcnt lgkmcnt(0)\n" \
  "v_mfma_f32_32x32x16_bf16 v[144:159], v[80:83], v[224:227], 0\n" \
  "v_cvt_pk_bf16_f32 v0, v96, v97\n" \
  "v_cvt_pk_bf16_f32 v1, v98, v99\n" \
  "v_cvt_pk_bf16_f32 v2, v100, v101\n" \
  "v_cvt_pk_bf16_f32 v3, v102, v103\n" \
  "v_cvt_pk_bf16_f32 v4, v104, v105\n" \
  "v_cvt_pk_bf16_f32 v5, v106, v107\n" \
  "v_mfma_f32_32x32x16_bf16 v[128:143], v[64:67], v[224:227], 0\n" \
  "s_waitcnt vmcnt(0)\n" \
  "ds_read_b128 v[80:83], v188 offset:0\n" \
  "v_cvt_pk_bf16_f32 v6, v108, v109\n" \
  "v_cvt_pk_bf16_f32 v7, v110, v111\n" \
  "v_cvt_pk_bf16_f32 v8, v112, v113\n" \
  "v_cvt_pk_bf16_f32 v9, v114, v115\n" \
  "v_cvt_pk_bf16_f32 v10, v116, v117\n" \
  "v_mfma_f32_32x32x16_bf16 v[144:159], v[84:87], v[228:231], v[144:159]\n" \
  "ds_read_b128 v[64:67], v240 offset:6144\n" \
  "v_cvt_pk_bf16_f32 v11, v118, v119\n" \
  "v_cvt_pk_bf16_f32 v12, v120, v121\n" \
  "v_cvt_pk_bf16_f32 v13, v122, v123\n" \
  "v_cvt_pk_bf16_f32 v14, v124, v125\n" \
  "v_cvt_pk_bf16_f32 v15, v126, v127\n" \
  "v_mfma_f32_32x32x16_bf16 v[128:143], v[68:71], v[228:231], v[128:143]\n" \
  "ds_read_b128 v[84:87], v189 offset:0\n" \
  "v_cvt_pk_bf16_f32 v16, v160, v161\n" \
  "v_cvt_pk_bf16_f32 v17, v162, v163\n" \
  "v_cvt_pk_bf16_f32 v18, v164, v165\n" \
  "v_cvt_pk_bf16_f32 v19, v166, v167\n" \
  "v_cvt_pk_bf16_f32 v20, v168, v169\n" \
  "v_mfma_f32_32x32x16_bf16 v[128:143], v[72:75], v[224:227], v[128:143]\n" \
  "ds_read_b128 v[68:71], v241 offset:6144\n" \
  "v_xor_b32 v212, 0x80008000, v212\n" \
  "v_xor_b32 v213, 0x80008000, v213\n" \
  "v_xor_b32 v214, 0x80008000, v214\n" \
  "v_xor_b32 v215, 0x80008000, v215\n" \
  "v_cvt_pk_bf16_f32 v21, v170, v171\n" \
  "v_mfma_f32_32x32x16_bf16 v[128:143], v[76:79], v[228:231], v[128:143]\n" \
  "ds_read_b128 v[72:75], v240 offset:40960\n" \
  "v_cvt_pk_bf16_f32 v22, v172, v173\n" \
  "v_cvt_pk_bf16_f32 v23, v174, v175\n" \
  "v_cvt_pk_bf16_f32 v24, v192, v193\n" \
  "v_cvt_pk_bf16_f32 v25, v194, v195\n" \
  "v_cvt_pk_bf16_f32 v26, v196, v197\n" \
  "v_mfma_f32_16x16x32_bf16 v[216:219], v[212:215], v[88:91], v[216:219]\n" \
  "ds_read_b128 v[76:79], v241 offset:40960\n" \
  "v_cvt_pk_bf16_f32 v27, v198, v199\n" \
  "v_mfma_f32_16x16x32_bf16 v[220:223], v[212:215], v[92:95], v[220:223]\n" \
  "v_cvt_pk_bf16_f32 v28, v200, v201\n" \
  "v_cvt_pk_bf16_f32 v29, v202, v203\n" \
  "v_mfma_f32_32x32x8bf16_1k v[96:111], v[208:209], v[184:185], 0\n" \
  "ds_read_b128 v[212:215], v246 offset:128\n" \
  "v_cvt_pk_bf16_f32 v30, v204, v205\n" \
  "v_cvt_pk_bf16_f32 v31, v206, v207\n" \
  "s_mov_b64 exec, 1\n" \
  "ds_inc_u32 v250, v250 offset:404\n" \
  "s_mov_b64 exec, -1\n" \
  "v_mfma_f32_32x32x8bf16_1k v[112:127], v[208:209], v[186:187], 0\n" \
  "ds_read_b64_tr_b16 v[88:89], v242 offset:40960\n" \
  "ds_read_b64_tr_b16 v[90:91], v244 offset:40960\n" \
  "ds_read_b64_tr_b16 v[92:93], v243 offset:40960\n" \
  "ds_read_b64_tr_b16 v[94:95], v245 offset:40960\n" \
  "v_mfma_f32_32x32x8bf16_1k v[160:175], v[210:211], v[184:185], 0\n" \
  "v_mfma_f32_32x32x8bf16_1k v[192:207], v[210:211], v[186:187], 0\n" \
  "v_pk_mul_f32 v[128:129], v[128:129], s[42:43] op_sel_hi:[1,0]\n" \
  "v_pk_mul_f32 v[130:131], v[130:131], s[42:43] op_sel_hi:[1,0]\n" \
  "v_pk_mul_f32 v[132:133], v[132:133], s[42:43] op_sel_hi:[1,0]\n" \
  "v_pk_mul_f32 v[134:135], v[134:135], s[42:43] op_sel_hi:[1,0]\n" \
  "v_pk_mul_f32 v[136:137], v[136:137], s[42:43] op_sel_hi:[1,0]\n" \
  "v_pk_mul_f32 v[138:139], v[138:139], s[42:43] op_sel_hi:[1,0]\n" \
  "v_pk_mul_f32 v[140:141], v[140:141], s[42:43] op_sel_hi:[1,0]\n" \
  "v_pk_mul_f32 v[142:143], v[142:143], s[42:43] op_sel_hi:[1,0]\n" \
  "v_pk_fma_f32 v[96:97], v[96:97], v[144:145], v[128:129]\n" \
  "v_pk_fma_f32 v[98:99], v[98:99], v[146:147], v[130:131]\n" \
  "v_pk_fma_f32 v[100:101], v[100:101], v[148:149], v[132:133]\n" \
  "v_pk_fma_f32 v[102:103], v[102:103], v[150:151], v[134:135]\n" \
  "v_pk_fma_f32 v[104:105], v[104:105], v[152:153], v[136:137]\n" \
  "v_pk_fma_f32 v[106:107], v[106:107], v[154:155], v[138:139]\n" \
  "v_pk_fma_f32 v[108:109], v[108:109], v[156:157], v[140:141]\n" \
  "v_pk_fma_f32 v[110:111], v[110:111], v[158:159], v[142:143]\n" \
  "v_pk_fma_f32 v[112:113], v[112:113], v[144:145], v[128:129]\n" \
  "v_pk_fma_f32 v[114:115], v[114:115], v[146:147], v[130:131]\n" \
  "v_pk_fma_f32 v[116:117], v[116:117], v[148:149], v[132:133]\n" \
  "v_pk_fma_f32 v[118:119], v[118:119], v[150:151], v[134:135]\n" \
  "v_pk_fma_f32 v[120:121], v[120:121], v[152:153], v[136:137]\n" \
  "v_pk_fma_f32 v[122:123], v[122:123], v[154:155], v[138:139]\n" \
  "v_pk_fma_f32 v[124:125], v[124:125], v[156:157], v[140:141]\n" \
  "v_pk_fma_f32 v[126:127], v[126:127], v[158:159], v[142:143]\n" \
  "v_pk_fma_f32 v[160:161], v[160:161], v[144:145], v[128:129]\n" \
  "v_pk_fma_f32 v[162:163], v[162:163], v[146:147], v[130:131]\n" \
  "v_pk_fma_f32 v[164:165], v[164:165], v[148:149], v[132:133]\n" \
  "v_pk_fma_f32 v[166:167], v[166:167], v[150:151], v[134:135]\n" \
  "v_pk_fma_f32 v[168:169], v[168:169], v[152:153], v[136:137]\n" \
  "v_pk_fma_f32 v[170:171], v[170:171], v[154:155], v[138:139]\n" \
  "v_pk_fma_f32 v[172:173], v[172:173], v[156:157], v[140:141]\n" \
  "v_pk_fma_f32 v[174:175], v[174:175], v[158:159], v[142:143]\n" \
  "v_pk_fma_f32 v[192:193], v[192:193], v[144:145], v[128:129]\n" \
  "v_pk_fma_f32 v[194:195], v[194:195], v[146:147], v[130:131]\n" \
  "v_pk_fma_f32 v[196:197], v[196:197], v[148:149], v[132:133]\n" \
  "v_pk_fma_f32 v[198:199], v[198:199], v[150:151], v[134:135]\n" \
  "v_pk_fma_f32 v[200:201], v[200:201], v[152:153], v[136:137]\n" \
  "v_pk_fma_f32 v[202:203], v[202:203], v[154:155], v[138:139]\n" \
  "v_pk_fma_f32 v[204:205], v[204:205], v[156:157], v[140:141]\n" \
  "v_pk_fma_f32 v[206:207], v[206:207], v[158:159], v[142:143]\n" \
  "s_waitcnt lgkmcnt(0)\n" \
  "v_mfma_f32_32x32x16_bf16 v[144:159], v[80:83], v[224:227], 0\n" \
  "v_cvt_pk_bf16_f32 v32, v96, v97\n" \
  "v_cvt_pk_bf16_f32 v33, v98, v99\n" \
  "v_cvt_pk_bf16_f32 v34, v100, v101\n" \
  "v_cvt_pk_bf16_f32 v35, v102, v103\n" \
  "v_cvt_pk_bf16_f32 v36, v104, v105\n" \
  "v_cvt_pk_bf16_f32 v37, v106, v107\n" \
  "v_mfma_f32_32x32x16_bf16 v[128:143], v[64:67], v[224:227], 0\n" \
  "v_cvt_pk_bf16_f32 v38, v108, v109\n" \
  "v_cvt_pk_bf16_f32 v39, v110, v111\n" \
  "v_cvt_pk_bf16_f32 v40, v112, v113\n" \
  "v_cvt_pk_bf16_f32 v41, v114, v115\n" \
  "v_cvt_pk_bf16_f32 v42, v116, v117\n" \
  "v_cvt_pk_bf16_f32 v43, v118, v119\n" \
  "v_mfma_f32_32x32x16_bf16 v[144:159], v[84:87], v[228:231], v[144:159]\n" \
  "v_cvt_pk_bf16_f32 v44, v120, v121\n" \
  "v_cvt_pk_bf16_f32 v45, v122, v123\n" \
  "v_cvt_pk_bf16_f32 v46, v124, v125\n" \
  "v_cvt_pk_bf16_f32 v47, v126, v127\n" \
  "v_cvt_pk_bf16_f32 v48, v160, v161\n" \
  "v_cvt_pk_bf16_f32 v49, v162, v163\n" \
  "v_mfma_f32_32x32x16_bf16 v[128:143], v[68:71], v[228:231], v[128:143]\n" \
  "ds_read_b32 v183, v255 offset:8\n" \
  "v_xor_b32 v212, 0x80008000, v212\n" \
  "v_xor_b32 v213, 0x80008000, v213\n" \
  "v_xor_b32 v214, 0x80008000, v214\n" \
  "v_xor_b32 v215, 0x80008000, v215\n" \
  "v_cvt_pk_bf16_f32 v50, v164, v165\n" \
  "v_mfma_f32_32x32x16_bf16 v[128:143], v[72:75], v[224:227], v[128:143]\n" \
  "v_cvt_pk_bf16_f32 v51, v166, v167\n" \
  "v_cvt_pk_bf16_f32 v52, v168, v169\n" \
  "v_cvt_pk_bf16_f32 v53, v170, v171\n" \
  "v_cvt_pk_bf16_f32 v54, v172, v173\n" \
  "v_cvt_pk_bf16_f32 v55, v174, v175\n" \
  "v_cvt_pk_bf16_f32 v56, v192, v193\n" \
  "v_mfma_f32_32x32x16_bf16 v[128:143], v[76:79], v[228:231], v[128:143]\n" \
  "v_cvt_pk_bf16_f32 v57, v194, v195\n" \
  "v_cvt_pk_bf16_f32 v58, v196, v197\n" \
  "v_cvt_pk_bf16_f32 v59, v198, v199\n" \
  "v_cvt_pk_bf16_f32 v60, v200, v201\n" \
  "v_cvt_pk_bf16_f32 v61, v202, v203\n" \
  "s_waitcnt lgkmcnt(0)\n" \
  "v_cmp_gt_u32 vcc, 4, v183\n" \
  "v_mfma_f32_16x16x32_bf16 v[216:219], v[212:215], v[88:91], v[216:219]\n" \
  "v_cvt_pk_bf16_f32 v62, v204, v205\n" \
  "v_cvt_pk_bf16_f32 v63, v206, v207\n" \
  "v_mfma_f32_16x16x32_bf16 v[220:223], v[212:215], v[92:95], v[220:223]\n" \
  "s_cbranch_vccnz Lw3%=\n" \
  "Lw3d%=:\n" \
  "v_mfma_f32_32x32x8bf16_1k v[96:111], v[232:233], v[184:185], 0\n" \
  "ds_read_b128 v[176:179], v240 offset:38912\n" \
  "ds_read_b128 v[180:183], v240 offset:34816\n" \
  "v_mfma_f32_32x32x8bf16_1k v[112:127], v[232:233], v[186:187], 0\n" \
  "ds_read_b128 v[188:191], v241 offset:38912\n" \
  "ds_read_b128 v[208:211], v241 offset:34816\n" \
  "v_mfma_f32_32x32x8bf16_1k v[160:175], v[234:235], v[184:185], 0\n" \
  "ds_read_b128 v[212:215], v246 offset:192\n" \
  "v_mfma_f32_32x32x8bf16_1k v[192:207], v[234:235], v[186:187], 0\n" \
  "ds_read_b128 v[232:235], v240 offset:36864\n" \
  "v_pk_mul_f32 v[128:129], v[128:129], s[42:43] op_sel_hi:[1,0]\n" \
  "v_pk_mul_f32 v[130:131], v[130:131], s[42:43] op_sel_hi:[1,0]\n" \
  "v_pk_mul_f32 v[132:133], v[132:133], s[42:43] op_sel_hi:[1,0]\n" \
  "v_pk_mul_f32 v[134:135], v[134:135], s[42:43] op_sel_hi:[1,0]\n" \
  "v_pk_mul_f32 v[136:137], v[136:137], s[42:43] op_sel_hi:[1,0]\n" \
  "v_pk_mul_f32 v[138:139], v[138:139], s[42:43] op_sel_hi:[1,0]\n" \
  "v_pk_mul_f32 v[140:141], v[140:141], s[42:43] op_sel_hi:[1,0]\n" \
  "v_pk_mul_f32 v[142:143], v[142:143], s[42:43] op_sel_hi:[1,0]\n" \
  "v_pk_fma_f32 v[96:97], v[96:97], v[144:145], v[128:129]\n" \
  "v_pk_fma_f32 v[98:99], v[98:99], v[146:147], v[130:131]\n" \
  "v_pk_fma_f32 v[100:101], v[100:101], v[148:149], v[132:133]\n" \
  "v_pk_fma_f32 v[102:103], v[102:103], v[150:151], v[134:135]\n" \
  "v_pk_fma_f32 v[104:105], v[104:105], v[152:153], v[136:137]\n" \
  "v_pk_fma_f32 v[106:107], v[106:107], v[154:155], v[138:139]\n" \
  "v_pk_fma_f32 v[108:109], v[108:109], v[156:157], v[140:141]\n" \
  "v_pk_fma_f32 v[110:111], v[110:111], v[158:159], v[142:143]\n" \
  "v_pk_fma_f32 v[112:113], v[112:113], v[144:145], v[128:129]\n" \
  "v_pk_fma_f32 v[114:115], v[114:115], v[146:147], v[130:131]\n" \
  "v_pk_fma_f32 v[116:117], v[116:117], v[148:149], v[132:133]\n" \
  "v_pk_fma_f32 v[118:119], v[118:119], v[150:151], v[134:135]\n" \
  "v_pk_fma_f32 v[120:121], v[120:121], v[152:153], v[136:137]\n" \
  "v_pk_fma_f32 v[122:123], v[122:123], v[154:155], v[138:139]\n" \
  "v_pk_fma_f32 v[124:125], v[124:125], v[156:157], v[140:141]\n" \
  "v_pk_fma_f32 v[126:127], v[126:127], v[158:159], v[142:143]\n" \
  "v_pk_fma_f32 v[160:161], v[160:161], v[144:145], v[128:129]\n" \
  "v_pk_fma_f32 v[162:163], v[162:163], v[146:147], v[130:131]\n" \
  "v_pk_fma_f32 v[164:165], v[164:165], v[148:149], v[132:133]\n" \
  "v_pk_fma_f32 v[166:167], v[166:167], v[150:151], v[134:135]\n" \
  "v_pk_fma_f32 v[168:169], v[168:169], v[152:153], v[136:137]\n" \
  "v_pk_fma_f32 v[170:171], v[170:171], v[154:155], v[138:139]\n" \
  "v_pk_fma_f32 v[172:173], v[172:173], v[156:157], v[140:141]\n" \
  "v_pk_fma_f32 v[174:175], v[174:175], v[158:159], v[142:143]\n" \
  "v_pk_fma_f32 v[192:193], v[192:193], v[144:145], v[128:129]\n" \
  "v_pk_fma_f32 v[194:195], v[194:195], v[146:147], v[130:131]\n" \
  "v_pk_fma_f32 v[196:197], v[196:197], v[148:149], v[132:133]\n" \
  "v_pk_fma_f32 v[198:199], v[198:199], v[150:151], v[134:135]\n" \
  "v_pk_fma_f32 v[200:201], v[200:201], v[152:153], v[136:137]\n" \
  "v_pk_fma_f32 v[202:203], v[202:203], v[154:155], v[138:139]\n" \
  "v_pk_fma_f32 v[204:205], v[204:205], v[156:157], v[140:141]\n" \
  "v_pk_fma_f32 v[206:207], v[206:207], v[158:159], v[142:143]\n" \
  "s_waitcnt lgkmcnt(5)\n" \
  "v_mfma_f32_32x32x16_bf16 v[144:159], v[176:179], v[224:227], 0\n" \
  "v_cvt_pk_bf16_f32 v80, v160, v161\n" \
  "v_cvt_pk_bf16_f32 v81, v162, v163\n" \
  "v_cvt_pk_bf16_f32 v82, v164, v165\n" \
  "v_cvt_pk_bf16_f32 v83, v166, v167\n" \
  "v_cvt_pk_bf16_f32 v84, v168, v169\n" \
  "v_cvt_pk_bf16_f32 v85, v170, v171\n" \
  "s_waitcnt lgkmcnt(4)\n" \
  "v_mfma_f32_32x32x16_bf16 v[128:143], v[180:183], v[224:227], 0\n" \
  "v_cvt_pk_bf16_f32 v86, v172, v173\n" \
  "v_cvt_pk_bf16_f32 v87, v174, v175\n" \
  "v_cvt_pk_bf16_f32 v88, v192, v193\n" \
  "v_cvt_pk_bf16_f32 v89, v194, v195\n" \
  "v_cvt_pk_bf16_f32 v90, v196, v197\n" \
  "v_cvt_pk_bf16_f32 v91, v198, v199\n" \
  "s_waitcnt lgkmcnt(3)\n" \
  "v_mfma_f32_32x32x16_bf16 v[144:159], v[188:191], v[228:231], v[144:159]\n" \
  "v_cvt_pk_bf16_f32 v92, v200, v201\n" \
  "v_cvt_pk_bf16_f32 v93, v202, v203\n" \
  "v_cvt_pk_bf16_f32 v94, v204, v205\n" \
  "v_cvt_pk_bf16_f32 v95, v206, v207\n" \
  "ds_read_b128 v[172:175], v241 offset:36864\n" \
  "ds_read_b64_tr_b16 v[200:201], v242 offset:36864\n" \
  "ds_read_b64_tr_b16 v[202:203], v244 offset:36864\n" \
  "ds_read_b64_tr_b16 v[204:205], v243 offset:36864\n" \
  "ds_read_b64_tr_b16 v[206:207], v245 offset:36864\n" \
  "s_mov_b64 exec, 1\n" \
  "ds_inc_u32 v250, v250 offset:408\n" \
  "s_mov_b64 exec, -1\n" \
  "s_waitcnt lgkmcnt(8)\n" \
  "v_mfma_f32_32x32x16_bf16 v[128:143], v[208:211], v[228:231], v[128:143]\n" \
  "v_cvt_pk_bf16_f32 v64, v96, v97\n" \
  "v_cvt_pk_bf16_f32 v65, v98, v99\n" \
  "v_cvt_pk_bf16_f32 v66, v100, v101\n" \
  "v_cvt_pk_bf16_f32 v67, v102, v103\n" \
  "v_cvt_pk_bf16_f32 v68, v104, v105\n" \
  "v_cvt_pk_bf16_f32 v69, v106, v107\n" \
  "s_waitcnt lgkmcnt(6)\n" \
  "v_mfma_f32_32x32x16_bf16 v[128:143], v[232:235], v[224:227], v[128:143]\n" \
  "v_cvt_pk_bf16_f32 v70, v108, v109\n" \
  "v_cvt_pk_bf16_f32 v71, v110, v111\n" \
  "v_xor_b32 v212, 0x80008000, v212\n" \
  "v_xor_b32 v213, 0x80008000, v213\n" \
  "v_xor_b32 v214, 0x80008000, v214\n" \
  "v_xor_b32 v215, 0x80008000, v215\n" \
  "v_mfma_f32_32x32x8bf16_1k v[96:111], v[236:237], v[184:185], 0\n" \
  "v_cvt_pk_bf16_f32 v72, v112, v113\n" \
  "v_cvt_pk_bf16_f32 v73, v114, v115\n" \
  "v_cvt_pk_bf16_f32 v74, v116, v117\n" \
  "v_cvt_pk_bf16_f32 v75, v118, v119\n" \
  "v_cvt_pk_bf16_f32 v76, v120, v121\n" \
  "v_cvt_pk_bf16_f32 v77, v122, v123\n" \
  "s_waitcnt lgkmcnt(5)\n" \
  "v_mfma_f32_32x32x16_bf16 v[128:143], v[172:175], v[228:231], v[128:143]\n" \
  "v_cvt_pk_bf16_f32 v78, v124, v125\n" \
  "v_cvt_pk_bf16_f32 v79, v126, v127\n" \
  "v_mfma_f32_32x32x8bf16_1k v[112:127], v[236:237], v[186:187], 0\n" \
  "s_waitcnt lgkmcnt(1)\n" \
  "v_mfma_f32_16x16x32_bf16 v[216:219], v[212:215], v[200:203], v[216:219]\n" \
  "v_mfma_f32_16x16x32_bf16 v[220:223], v[212:215], v[204:207], v[220:223]\n" \
  "v_mfma_f32_32x32x8bf16_1k v[192:207], v[238:239], v[186:187], 0\n" \
  "v_mfma_f32_32x32x8bf16_1k v[160:175], v[238:239], v[184:185], 0\n" \
  "s_nop 7\n" \
  "v_pk_mul_f32 v[128:129], v[128:129], s[42:43] op_sel_hi:[1,0]\n" \
  "v_pk_mul_f32 v[130:131], v[130:131], s[42:43] op_sel_hi:[1,0]\n" \
  "v_pk_mul_f32 v[132:133], v[132:133], s[42:43] op_sel_hi:[1,0]\n" \
  "v_pk_mul_f32 v[134:135], v[134:135], s[42:43] op_sel_hi:[1,0]\n" \
  "v_pk_mul_f32 v[136:137], v[136:137], s[42:43] op_sel_hi:[1,0]\n" \
  "v_pk_mul_f32 v[138:139], v[138:139], s[42:43] op_sel_hi:[1,0]\n" \
  "v_pk_mul_f32 v[140:141], v[140:141], s[42:43] op_sel_hi:[1,0]\n" \
  "v_pk_mul_f32 v[142:143], v[142:143], s[42:43] op_sel_hi:[1,0]\n" \
  "v_pk_fma_f32 v[96:97], v[96:97], v[144:145], v[128:129]\n" \
  "v_pk_fma_f32 v[98:99], v[98:99], v[146:147], v[130:131]\n" \
  "v_pk_fma_f32 v[100:101], v[100:101], v[148:149], v[132:133]\n" \
  "v_pk_fma_f32 v[102:103], v[102:103], v[150:151], v[134:135]\n" \
  "v_pk_fma_f32 v[104:105], v[104:105], v[152:153], v[136:137]\n" \
  "v_pk_fma_f32 v[106:107], v[106:107], v[154:155], v[138:139]\n" \
  "v_pk_fma_f32 v[108:109], v[108:109], v[156:157], v[140:141]\n" \
  "v_pk_fma_f32 v[110:111], v[110:111], v[158:159], v[142:143]\n" \
  "v_pk_fma_f32 v[112:113], v[112:113], v[144:145], v[128:129]\n" \
  "v_pk_fma_f32 v[114:115], v[114:115], v[146:147], v[130:131]\n" \
  "v_pk_fma_f32 v[116:117], v[116:117], v[148:149], v[132:133]\n" \
  "v_pk_fma_f32 v[118:119], v[118:119], v[150:151], v[134:135]\n" \
  "v_pk_fma_f32 v[120:121], v[120:121], v[152:153], v[136:137]\n" \
  "v_pk_fma_f32 v[122:123], v[122:123], v[154:155], v[138:139]\n" \
  "v_pk_fma_f32 v[124:125], v[124:125], v[156:157], v[140:141]\n" \
  "v_pk_fma_f32 v[126:127], v[126:127], v[158:159], v[142:143]\n" \
  "v_pk_fma_f32 v[160:161], v[160:161], v[144:145], v[128:129]\n" \
  "v_pk_fma_f32 v[162:163], v[162:163], v[146:147], v[130:131]\n" \
  "v_pk_fma_f32 v[164:165], v[164:165], v[148:149], v[132:133]\n" \
  "v_pk_fma_f32 v[166:167], v[166:167], v[150:151], v[134:135]\n" \
  "v_pk_fma_f32 v[168:169], v[168:169], v[152:153], v[136:137]\n" \
  "v_pk_fma_f32 v[170:171], v[170:171], v[154:155], v[138:139]\n" \
  "v_pk_fma_f32 v[172:173], v[172:173], v[156:157], v[140:141]\n" \
  "v_pk_fma_f32 v[174:175], v[174:175], v[158:159], v[142:143]\n" \
  "v_pk_fma_f32 v[192:193], v[192:193], v[144:145], v[128:129]\n" \
  "v_pk_fma_f32 v[194:195], v[194:195], v[146:147], v[130:131]\n" \
  "v_pk_fma_f32 v[196:197], v[196:197], v[148:149], v[132:133]\n" \
  "v_pk_fma_f32 v[198:199], v[198:199], v[150:151], v[134:135]\n" \
  "v_pk_fma_f32 v[200:201], v[200:201], v[152:153], v[136:137]\n" \
  "v_pk_fma_f32 v[202:203], v[202:203], v[154:155], v[138:139]\n" \
  "v_pk_fma_f32 v[204:205], v[204:205], v[156:157], v[140:141]\n" \
  "v_pk_fma_f32 v[206:207], v[206:207], v[158:159], v[142:143]\n" \
  "ds_read_b32 v208, v250 offset:416\n" \
  "ds_read_b32 v209, v250 offset:32\n" \
  "ds_read_b32 v210, v255 offset:4\n" \
  "ds_read_b128 v[232:235], v250 offset:432\n" \
  "ds_read_b128 v[212:215], v248\n" \
  "v_cvt_pk_bf16_f32 v96, v96, v97\n" \
  "v_cvt_pk_bf16_f32 v97, v98, v99\n" \
  "v_cvt_pk_bf16_f32 v98, v100, v101\n" \
  "v_cvt_pk_bf16_f32 v99, v102, v103\n" \
  "v_cvt_pk_bf16_f32 v100, v104, v105\n" \
  "v_cvt_pk_bf16_f32 v101, v106, v107\n" \
  "v_cvt_pk_bf16_f32 v102, v108, v109\n" \
  "v_cvt_pk_bf16_f32 v103, v110, v111\n" \
  "v_mov_b32 v224, v251\n" \
  "v_xor_b32 v225, 32, v251\n" \
  "v_xor_b32 v226, 64, v251\n" \
  "v_xor_b32 v227, 96, v251\n" \
  "v_xor_b32 v228, 128, v251\n" \
  "v_xor_b32 v229, 160, v251\n" \
  "v_xor_b32 v230, 192, v251\n" \
  "v_xor_b32 v231, 224, v251\n" \
  "s_mov_b32 s47, 0\n" \
  "Lchk%=:\n" \
  "s_waitcnt lgkmcnt(0)\n" \
  "v_min3_u32 v209, v208, v209, v210\n" \
  "v_max_u32_e64 v232, v232, v233\n" \
  "v_max3_u32 v232, v232, v234, v235\n" \
  "v_cmp_gt_u32 vcc, 4, v209\n" \
  "s_mov_b32 s52, 0xffff0000\n" \
  "s_mov_b32 s53, 0xffff0000\n" \
  "s_mov_b32 s44, 0\n" \
  "v_readfirstlane_b32 s45, v232\n" \
  "s_cbranch_vccnz Lcr%=\n" \
  "ds_read_b32 v237, v250 offset:36\n" \
  "ds_read_b128 v[144:147], v224 offset:0\n" \
  "ds_read_b128 v[148:151], v225 offset:0\n" \
  "ds_read_b128 v[152:155], v226 offset:0\n" \
  "ds_read_b128 v[156:159], v227 offset:0\n" \
  "ds_read_b128 v[176:179], v249 offset:0\n" \
  "ds_read_b128 v[180:183], v249 offset:32\n" \
  "ds_read_b128 v[184:187], v249 offset:64\n" \
  "ds_read_b128 v[188:191], v249 offset:96\n" \
  "v_cvt_pk_bf16_f32 v104, v112, v113\n" \
  "v_cvt_pk_bf16_f32 v105, v114, v115\n" \
  "v_cvt_pk_bf16_f32 v106, v116, v117\n" \
  "v_cvt_pk_bf16_f32 v107, v118, v119\n" \
  "v_cvt_pk_bf16_f32 v108, v120, v121\n" \
  "v_cvt_pk_bf16_f32 v109, v122, v123\n" \
  "v_cvt_pk_bf16_f32 v110, v124, v125\n" \
  "v_cvt_pk_bf16_f32 v111, v126, v127\n" \
  "s_waitcnt lgkmcnt(7)\n" \
  "v_mfma_f32_32x32x16_bf16 v[128:143], v[0:3], v[144:147], 0\n" \
  "v_cmp_gt_u32 vcc, 4, v237\n" \
  "ds_read_b128 v[144:147], v228 offset:0\n" \
  "v_cvt_pk_bf16_f32 v112, v160, v161\n" \
  "v_cvt_pk_bf16_f32 v113, v162, v163\n" \
  "v_cvt_pk_bf16_f32 v114, v164, v165\n" \
  "v_cvt_pk_bf16_f32 v115, v166, v167\n" \
  "v_cvt_pk_bf16_f32 v116, v168, v169\n" \
  "v_cvt_pk_bf16_f32 v117, v170, v171\n" \
  "s_waitcnt lgkmcnt(7)\n" \
  "v_mfma_f32_32x32x16_bf16 v[128:143], v[4:7], v[148:151], v[128:143]\n" \
  "ds_read_b128 v[148:151], v229 offset:0\n" \
  "v_cvt_pk_bf16_f32 v118, v172, v173\n" \
  "v_cvt_pk_bf16_f32 v119, v174, v175\n" \
  "v_cvt_pk_bf16_f32 v120, v192, v193\n" \
  "v_cvt_pk_bf16_f32 v121, v194, v195\n" \
  "v_cvt_pk_bf16_f32 v122, v196, v197\n" \
  "v_cvt_pk_bf16_f32 v123, v198, v199\n" \
  "s_waitcnt lgkmcnt(7)\n" \
  "v_mfma_f32_32x32x16_bf16 v[128:143], v[32:35], v[152:155], v[128:143]\n" \
  "ds_read_b128 v[152:155], v230 offset:0\n" \
  "v_cvt_pk_bf16_f32 v124, v200, v201\n" \
  "v_cvt_pk_bf16_f32 v125, v202, v203\n" \
  "v_cvt_pk_bf16_f32 v126, v204, v205\n" \
  "v_cvt_pk_bf16_f32 v127, v206, v207\n" \
  "v_cndmask_b32 v216, v216, v220, s[52:53]\n" \
  "v_cndmask_b32 v217, v217, v221, s[52:53]\n" \
  "s_waitcnt lgkmcnt(7)\n" \
  "v_mfma_f32_32x32x16_bf16 v[128:143], v[36:39], v[156:159], v[128:143]\n" \
  "ds_read_b32 v204, v249 offset:256\n" \
  "ds_read_b128 v[156:159], v231 offset:0\n" \
  "v_cndmask_b32 v218, v218, v222, s[52:53]\n" \
  "v_cndmask_b32 v219, v219, v223, s[52:53]\n" \
  "v_add_f32 v216, v216, v212\n" \
  "v_add_f32 v217, v217, v213\n" \
  "v_add_f32 v218, v218, v214\n" \
  "v_add_f32_e64 v219, v219, v215\n" \
  "s_cbranch_vccnz Lspp%=\n" \
  "Lgop%=:\n" \
  "s_waitcnt lgkmcnt(4)\n" \
  "v_mfma_f32_32x32x16_bf16 v[128:143], v[64:67], v[144:147], v[128:143]\n" \
  "ds_read_b128 v[160:163], v224 offset:8704\n" \
  "v_mul_f32 v216, v247, v216\n" \
  "v_mul_f32 v217, v247, v217\n" \
  "v_mul_f32 v218, v247, v218\n" \
  "v_mul_f32_e64 v219, v247, v219\n" \
  "v_and_b32 v220, 0xffff0000, v216\n" \
  "v_and_b32 v221, 0xffff0000, v217\n" \
  "s_waitcnt lgkmcnt(4)\n" \
  "v_mfma_f32_32x32x16_bf16 v[128:143], v[68:71], v[148:151], v[128:143]\n" \
  "ds_read_b128 v[164:167], v225 offset:8704\n" \
  "v_and_b32 v222, 0xffff0000, v218\n" \
  "v_and_b32 v223, 0xffff0000, v219\n" \
  "v_sub_f32 v216, v216, v220\n" \
  "v_sub_f32 v217, v217, v221\n" \
  "v_sub_f32 v218, v218, v222\n" \
  "v_sub_f32_e64 v219, v219, v223\n" \
  "v_cvt_pk_bf16_f32 v208, v220, v216\n" \
  "s_waitcnt lgkmcnt(4)\n" \
  "v_mfma_f32_32x32x16_bf16 v[128:143], v[96:99], v[152:155], v[128:143]\n" \
  "ds_read_b128 v[168:171], v226 offset:8704\n" \
  "v_mov_b32 v209, 0xc1f0\n" \
  "v_cvt_pk_bf16_f32 v210, v221, v217\n" \
  "v_mov_b32 v211, 0xc1f0\n" \
  "v_cvt_pk_bf16_f32 v212, v222, v218\n" \
  "v_mov_b32 v213, 0xc1f0\n" \
  "v_cvt_pk_bf16_f32 v214, v223, v219\n" \
  "v_mov_b32 v215, 0xc1f0\n" \
  "s_waitcnt lgkmcnt(3)\n" \
  "v_mfma_f32_32x32x16_bf16 v[128:143], v[100:103], v[156:159], v[128:143]\n" \
  "ds_read_b128 v[172:175], v227 offset:8704\n" \
  "v_mov_b32 v216, 0x3f803f80\n" \
  "v_mov_b32 v217, 0x3f80\n" \
  "s_mov_b32 exec_lo, 0\n" \
  "v_mov_b32 v216, 0\n" \
  "v_mov_b32 v217, 0\n" \
  "s_mov_b32 exec_lo, -1\n" \
  "s_mov_b32 s52, 0xfffe6800\n" \
  "v_mov_b32 v205, 0\n" \
  "v_add_u32 v233, 104448, v252\n" \
  "v_mfma_f32_32x32x8bf16_1k v[128:143], v[208:209], v[216:217], v[128:143]\n" \
  "s_branch Lph1m%=\n" \
  ".p2align 6\n" \
  "Lph0%=:\n" \
  "v_pk_add_f32 v[128:129], v[128:129], v[176:177] clamp\n" \
  "v_pk_fma_f32 v[222:223], v[196:197], v[192:193], v[204:205]\n" \
  "v_pk_add_f32 v[130:131], v[130:131], v[178:179] clamp\n" \
  "v_pk_add_f32 v[136:137], v[136:137], v[184:185] clamp\n" \
  "v_pk_fma_f32 v[222:223], v[198:199], v[194:195], v[222:223]\n" \
  "v_pk_add_f32 v[138:139], v[138:139], v[186:187] clamp\n" \
  "v_pk_add_f32 v[132:133], v[132:133], v[180:181] clamp\n" \
  "v_pk_add_f32 v[134:135], v[134:135], v[182:183] clamp\n" \
  "v_pk_add_f32 v[140:141], v[140:141], v[188:189] clamp\n" \
  "v_pk_add_f32 v[142:143], v[142:143], v[190:191] clamp\n" \
  "v_pk_add_f32 v[200:201], v[128:129], v[130:131]\n" \
  "v_pk_add_f32 v[202:203], v[136:137], v[138:139]\n" \
  "v_pk_mul_f32 v[128:129], v[128:129], v[130:131]\n" \
  "v_pk_mul_f32 v[136:137], v[136:137], v[138:139]\n" \
  "v_pk_add_f32 v[130:131], v[132:133], v[134:135]\n" \
  "v_pk_add_f32 v[138:139], v[140:141], v[142:143]\n" \
  "v_pk_mul_f32 v[132:133], v[132:133], v[134:135]\n" \
  "v_pk_mul_f32 v[140:141], v[140:141], v[142:143]\n" \
  "v_pk_mul_f32 v[196:197], v[130:131], v[128:129]\n" \
  "v_pk_mul_f32 v[198:199], v[138:139], v[136:137]\n" \
  "v_pk_mul_f32 v[192:193], v[128:129], v[132:133]\n" \
  "v_pk_mul_f32 v[194:195], v[136:137], v[140:141]\n" \
  "v_pk_fma_f32 v[196:197], v[200:201], v[132:133], v[196:197]\n" \
  "v_pk_fma_f32 v[198:199], v[202:203], v[140:141], v[198:199]\n" \
  "v_mfma_f32_32x32x8bf16_1k v[128:143], v[208:209], v[216:217], 0\n" \
  "s_waitcnt lgkmcnt(3)\n" \
  "ds_read_b32 v237, v250 offset:36\n" \
  "v_rcp_f32 v192, v192\n" \
  "v_add_f32 v222, v222, v223\n" \
  "v_rcp_f32 v193, v193\n" \
  "v_mfma_f32_32x32x16_bf16 v[128:143], v[0:3], v[160:163], v[128:143]\n" \
  "s_waitcnt lgkmcnt(3)\n" \
  "ds_read_b128 v[160:163], v228 offset:0\n" \
  "v_rcp_f32 v194, v194\n" \
  "v_mul_f32 v222, v222, v219\n" \
  "v_rcp_f32 v195, v195\n" \
  "v_mfma_f32_32x32x16_bf16 v[128:143], v[4:7], v[164:167], v[128:143]\n" \
  "s_waitcnt lgkmcnt(3)\n" \
  "ds_read_b128 v[164:167], v229 offset:0\n" \
  "v_exp_f32 v144, v144\n" \
  "v_cvt_pk_bf16_f32 v239, v222, v222\n" \
  "v_exp_f32_e64 v145, v145\n" \
  "v_mfma_f32_32x32x16_bf16 v[128:143], v[32:35], v[168:171], v[128:143]\n" \
  "s_waitcnt lgkmcnt(2)\n" \
  "ds_read_b128 v[168:171], v230 offset:0\n" \
  "v_cmp_gt_u32 vcc, 4, v237\n" \
  "v_exp_f32 v146, v146\n" \
  "v_exp_f32 v147, v147\n" \
  "v_mfma_f32_32x32x16_bf16 v[128:143], v[36:39], v[172:175], v[128:143]\n" \
  "s_waitcnt lgkmcnt(2)\n" \
  "ds_read_b128 v[172:175], v231 offset:0\n" \
  "v_exp_f32 v148, v148\n" \
  "v_exp_f32 v149, v149\n" \
  "s_cbranch_vccnz Lsp0%=\n" \
  "Lgo0%=:\n" \
  "v_mfma_f32_32x32x16_bf16 v[128:143], v[64:67], v[160:163], v[128:143]\n" \
  "s_waitcnt lgkmcnt(2)\n" \
  "ds_read_b128 v[160:163], v224 offset:8704\n" \
  "ds_write_b16 v233, v239 offset:8704\n" \
  "v_exp_f32 v150, v150\n" \
  "v_exp_f32_e64 v151, v151\n" \
  "v_mfma_f32_32x32x16_bf16 v[128:143], v[68:71], v[164:167], v[128:143]\n" \
  "s_waitcnt lgkmcnt(3)\n" \
  "ds_read_b128 v[164:167], v225 offset:8704\n" \
  "v_exp_f32 v152, v152\n" \
  "v_exp_f32 v153, v153\n" \
  "v_add_u32 v254, 16, v254\n" \
  "v_mfma_f32_32x32x16_bf16 v[128:143], v[96:99], v[168:171], v[128:143]\n" \
  "s_waitcnt lgkmcnt(3)\n" \
  "ds_read_b128 v[168:171], v226 offset:8704\n" \
  "v_exp_f32 v154, v154\n" \
  "v_exp_f32_e64 v155, v155\n" \
  "v_add_u32 v253, 512, v253\n" \
  "v_mfma_f32_32x32x16_bf16 v[128:143], v[100:103], v[172:175], v[128:143]\n" \
  "ds_read_b128 v[172:175], v227 offset:8704\n" \
  "v_exp_f32 v156, v156\n" \
  "v_exp_f32 v157, v157\n" \
  "v_exp_f32 v158, v158\n" \
  "v_exp_f32 v159, v159\n" \
  "Lph1%=:\n" \
  "v_pk_add_f32 v[144:145], v[144:145], v[176:177] clamp\n" \
  "v_pk_fma_f32 v[222:223], v[196:197], v[192:193], v[204:205]\n" \
  "v_pk_add_f32 v[146:147], v[146:147], v[178:179] clamp\n" \
  "v_pk_add_f32 v[152:153], v[152:153], v[184:185] clamp\n" \
  "v_pk_fma_f32 v[222:223], v[198:199], v[194:195], v[222:223]\n" \
  "v_pk_add_f32 v[154:155], v[154:155], v[186:187] clamp\n" \
  "v_pk_add_f32 v[148:149], v[148:149], v[180:181] clamp\n" \
  "v_pk_add_f32 v[150:151], v[150:151], v[182:183] clamp\n" \
  "v_pk_add_f32 v[156:157], v[156:157], v[188:189] clamp\n" \
  "v_pk_add_f32 v[158:159], v[158:159], v[190:191] clamp\n" \
  "v_pk_add_f32 v[200:201], v[144:145], v[146:147]\n" \
  "v_pk_add_f32 v[202:203], v[152:153], v[154:155]\n" \
  "v_pk_mul_f32 v[144:145], v[144:145], v[146:147]\n" \
  "v_pk_mul_f32 v[152:153], v[152:153], v[154:155]\n" \
  "v_pk_add_f32 v[146:147], v[148:149], v[150:151]\n" \
  "v_pk_add_f32 v[154:155], v[156:157], v[158:159]\n" \
  "v_pk_mul_f32 v[148:149], v[148:149], v[150:151]\n" \
  "v_pk_mul_f32 v[156:157], v[156:157], v[158:159]\n" \
  "v_pk_mul_f32 v[196:197], v[146:147], v[144:145]\n" \
  "v_pk_mul_f32 v[198:199], v[154:155], v[152:153]\n" \
  "v_pk_mul_f32 v[192:193], v[144:145], v[148:149]\n" \
  "v_pk_mul_f32 v[194:195], v[152:153], v[156:157]\n" \
  "v_pk_fma_f32 v[196:197], v[200:201], v[148:149], v[196:197]\n" \
  "v_pk_fma_f32 v[198:199], v[202:203], v[156:157], v[198:199]\n" \
  "Lph1m%=:\n" \
  "v_mfma_f32_32x32x8bf16_1k v[144:159], v[210:211], v[216:217], 0\n" \
  "s_waitcnt lgkmcnt(3)\n" \
  "ds_read_b32 v237, v250 offset:40\n" \
  "v_rcp_f32 v192, v192\n" \
  "v_add_f32 v222, v222, v223\n" \
  "v_rcp_f32 v193, v193\n" \
  "v_mfma_f32_32x32x16_bf16 v[144:159], v[8:11], v[160:163], v[144:159]\n" \
  "s_waitcnt lgkmcnt(3)\n" \
  "ds_read_b128 v[160:163], v228 offset:8704\n" \
  "v_rcp_f32 v194, v194\n" \
  "v_mul_f32 v222, v222, v220\n" \
  "v_rcp_f32 v195, v195\n" \
  "v_mfma_f32_32x32x16_bf16 v[144:159], v[12:15], v[164:167], v[144:159]\n" \
  "s_waitcnt lgkmcnt(3)\n" \
  "ds_read_b128 v[164:167], v229 offset:8704\n" \
  "v_exp_f32 v128, v128\n" \
  "v_cvt_pk_bf16_f32 v239, v222, v222\n" \
  "v_exp_f32_e64 v129, v129\n" \
  "v_mfma_f32_32x32x16_bf16 v[144:159], v[40:43], v[168:171], v[144:159]\n" \
  "s_waitcnt lgkmcnt(2)\n" \
  "ds_read_b128 v[168:171], v230 offset:8704\n" \
  "v_cmp_gt_u32 vcc, 4, v237\n" \
  "v_exp_f32 v130, v130\n" \
  "v_exp_f32 v131, v131\n" \
  "v_mfma_f32_32x32x16_bf16 v[144:159], v[44:47], v[172:175], v[144:159]\n" \
  "s_waitcnt lgkmcnt(2)\n" \
  "ds_read_b128 v[172:175], v231 offset:8704\n" \
  "s_add_u32 s44, s44, 1\n" \
  "v_exp_f32 v132, v132\n" \
  "v_exp_f32_e64 v133, v133\n" \
  "s_cbranch_vccnz Lsp1%=\n" \
  "Lgo1%=:\n" \
  "v_mfma_f32_32x32x16_bf16 v[144:159], v[72:75], v[160:163], v[144:159]\n" \
  "s_waitcnt lgkmcnt(2)\n" \
  "ds_read_b128 v[160:163], v224 offset:17408\n" \
  "ds_write_b16 v233, v239 offset:17408\n" \
  "v_exp_f32 v134, v134\n" \
  "v_exp_f32_e64 v135, v135\n" \
  "v_mfma_f32_32x32x16_bf16 v[144:159], v[76:79], v[164:167], v[144:159]\n" \
  "s_waitcnt lgkmcnt(3)\n" \
  "ds_read_b128 v[164:167], v225 offset:17408\n" \
  "s_and_b32 s47, s44, 3\n" \
  "v_exp_f32 v136, v136\n" \
  "v_exp_f32 v137, v137\n" \
  "v_add_u32_e64 v250, 16, v250\n" \
  "v_mfma_f32_32x32x16_bf16 v[144:159], v[104:107], v[168:171], v[144:159]\n" \
  "s_waitcnt lgkmcnt(3)\n" \
  "ds_read_b128 v[168:171], v226 offset:17408\n" \
  "s_cmp_eq_u32 s47, 0\n" \
  "s_cselect_b32 s46, s52, 34816\n" \
  "v_exp_f32 v138, v138\n" \
  "v_exp_f32 v139, v139\n" \
  "v_mfma_f32_32x32x16_bf16 v[144:159], v[108:111], v[172:175], v[144:159]\n" \
  "ds_read_b128 v[172:175], v227 offset:17408\n" \
  "v_exp_f32 v140, v140\n" \
  "v_exp_f32 v141, v141\n" \
  "v_exp_f32 v142, v142\n" \
  "v_exp_f32 v143, v143\n" \
  "Lph2%=:\n" \
  "v_pk_add_f32 v[128:129], v[128:129], v[176:177] clamp\n" \
  "v_pk_fma_f32 v[222:223], v[196:197], v[192:193], v[204:205]\n" \
  "v_pk_add_f32 v[130:131], v[130:131], v[178:179] clamp\n" \
  "v_pk_add_f32 v[136:137], v[136:137], v[184:185] clamp\n" \
  "v_pk_fma_f32 v[222:223], v[198:199], v[194:195], v[222:223]\n" \
  "v_pk_add_f32 v[138:139], v[138:139], v[186:187] clamp\n" \
  "v_pk_add_f32 v[132:133], v[132:133], v[180:181] clamp\n" \
  "v_pk_add_f32 v[134:135], v[134:135], v[182:183] clamp\n" \
  "v_pk_add_f32 v[140:141], v[140:141], v[188:189] clamp\n" \
  "v_pk_add_f32 v[142:143], v[142:143], v[190:191] clamp\n" \
  "v_pk_add_f32 v[200:201], v[128:129], v[130:131]\n" \
  "v_pk_add_f32 v[202:203], v[136:137], v[138:139]\n" \
  "v_pk_mul_f32 v[128:129], v[128:129], v[130:131]\n" \
  "v_pk_mul_f32 v[136:137], v[136:137], v[138:139]\n" \
  "v_pk_add_f32 v[130:131], v[132:133], v[134:135]\n" \
  "v_pk_add_f32 v[138:139], v[140:141], v[142:143]\n" \
  "v_pk_mul_f32 v[132:133], v[132:133], v[134:135]\n" \
  "v_pk_mul_f32 v[140:141], v[140:141], v[142:143]\n" \
  "v_pk_mul_f32 v[196:197], v[130:131], v[128:129]\n" \
  "v_pk_mul_f32 v[198:199], v[138:139], v[136:137]\n" \
  "v_pk_mul_f32 v[192:193], v[128:129], v[132:133]\n" \
  "v_pk_mul_f32 v[194:195], v[136:137], v[140:141]\n" \
  "v_pk_fma_f32 v[196:197], v[200:201], v[132:133], v[196:197]\n" \
  "v_pk_fma_f32 v[198:199], v[202:203], v[140:141], v[198:199]\n" \
  "v_mfma_f32_32x32x8bf16_1k v[128:143], v[212:213], v[216:217], 0\n" \
  "s_waitcnt lgkmcnt(3)\n" \
  "ds_read_b32 v237, v250 offset:28\n" \
  "v_rcp_f32 v192, v192\n" \
  "v_add_f32 v222, v222, v223\n" \
  "v_rcp_f32 v193, v193\n" \
  "v_mfma_f32_32x32x16_bf16 v[128:143], v[16:19], v[160:163], v[128:143]\n" \
  "s_waitcnt lgkmcnt(3)\n" \
  "ds_read_b128 v[160:163], v228 offset:17408\n" \
  "v_rcp_f32 v194, v194\n" \
  "v_mul_f32 v222, v222, v221\n" \
  "v_rcp_f32 v195, v195\n" \
  "v_mfma_f32_32x32x16_bf16 v[128:143], v[20:23], v[164:167], v[128:143]\n" \
  "s_waitcnt lgkmcnt(3)\n" \
  "ds_read_b128 v[164:167], v229 offset:17408\n" \
  "v_exp_f32 v144, v144\n" \
  "v_cvt_pk_bf16_f32 v239, v222, v222\n" \
  "v_exp_f32_e64 v145, v145\n" \
  "v_mfma_f32_32x32x16_bf16 v[128:143], v[48:51], v[168:171], v[128:143]\n" \
  "s_waitcnt lgkmcnt(2)\n" \
  "ds_read_b128 v[168:171], v230 offset:17408\n" \
  "v_cmp_gt_u32 vcc, 4, v237\n" \
  "v_exp_f32 v146, v146\n" \
  "v_exp_f32 v147, v147\n" \
  "v_mfma_f32_32x32x16_bf16 v[128:143], v[52:55], v[172:175], v[128:143]\n" \
  "s_waitcnt lgkmcnt(2)\n" \
  "ds_read_b128 v[172:175], v231 offset:17408\n" \
  "v_exp_f32 v148, v148\n" \
  "v_exp_f32 v149, v149\n" \
  "s_cbranch_vccnz Lsp2%=\n" \
  "Lgo2%=:\n" \
  "v_mfma_f32_32x32x16_bf16 v[128:143], v[80:83], v[160:163], v[128:143]\n" \
  "s_waitcnt lgkmcnt(2)\n" \
  "ds_read_b128 v[160:163], v224 offset:26112\n" \
  "ds_write_b16 v233, v239 offset:26112\n" \
  "v_exp_f32 v150, v150\n" \
  "v_exp_f32_e64 v151, v151\n" \
  "v_mfma_f32_32x32x16_bf16 v[128:143], v[84:87], v[164:167], v[128:143]\n" \
  "s_waitcnt lgkmcnt(3)\n" \
  "ds_read_b128 v[164:167], v225 offset:26112\n" \
  "s_mov_b64 exec, 15\n" \
  "ds_inc_u32 v254, v254\n" \
  "s_mov_b64 exec, -1\n" \
  "ds_read_b128 v[218:221], v253 offset:0\n" \
  "v_exp_f32 v152, v152\n" \
  "v_exp_f32 v153, v153\n" \
  "v_add_u32 v224, s46, v224\n" \
  "v_mfma_f32_32x32x16_bf16 v[128:143], v[112:115], v[168:171], v[128:143]\n" \
  "s_waitcnt lgkmcnt(5)\n" \
  "ds_read_b128 v[168:171], v226 offset:26112\n" \
  "v_exp_f32 v154, v154\n" \
  "v_exp_f32 v155, v155\n" \
  "v_add_u32 v225, s46, v225\n" \
  "v_mfma_f32_32x32x16_bf16 v[128:143], v[116:119], v[172:175], v[128:143]\n" \
  "ds_read_b128 v[172:175], v227 offset:26112\n" \
  "v_exp_f32 v156, v156\n" \
  "v_exp_f32 v157, v157\n" \
  "v_exp_f32 v158, v158\n" \
  "v_exp_f32 v159, v159\n" \
  "Lph3%=:\n" \
  "v_pk_add_f32 v[144:145], v[144:145], v[176:177] clamp\n" \
  "v_pk_fma_f32 v[222:223], v[196:197], v[192:193], v[204:205]\n" \
  "v_pk_add_f32 v[146:147], v[146:147], v[178:179] clamp\n" \
  "v_pk_add_f32 v[152:153], v[152:153], v[184:185] clamp\n" \
  "v_pk_fma_f32 v[222:223], v[198:199], v[194:195], v[222:223]\n" \
  "v_pk_add_f32 v[154:155], v[154:155], v[186:187] clamp\n" \
  "v_pk_add_f32 v[148:149], v[148:149], v[180:181] clamp\n" \
  "v_pk_add_f32 v[150:151], v[150:151], v[182:183] clamp\n" \
  "v_pk_add_f32 v[156:157], v[156:157], v[188:189] clamp\n" \
  "v_pk_add_f32 v[158:159], v[158:159], v[190:191] clamp\n" \
  "v_pk_add_f32 v[200:201], v[144:145], v[146:147]\n" \
  "v_pk_add_f32 v[202:203], v[152:153], v[154:155]\n" \
  "v_pk_mul_f32 v[144:145], v[144:145], v[146:147]\n" \
  "v_pk_mul_f32 v[152:153], v[152:153], v[154:155]\n" \
  "v_pk_add_f32 v[146:147], v[148:149], v[150:151]\n" \
  "v_pk_add_f32 v[154:155], v[156:157], v[158:159]\n" \
  "v_pk_mul_f32 v[148:149], v[148:149], v[150:151]\n" \
  "v_pk_mul_f32 v[156:157], v[156:157], v[158:159]\n" \
  "v_pk_mul_f32 v[196:197], v[146:147], v[144:145]\n" \
  "v_pk_mul_f32 v[198:199], v[154:155], v[152:153]\n" \
  "v_pk_mul_f32 v[192:193], v[144:145], v[148:149]\n" \
  "v_pk_mul_f32 v[194:195], v[152:153], v[156:157]\n" \
  "v_pk_fma_f32 v[196:197], v[200:201], v[148:149], v[196:197]\n" \
  "v_pk_fma_f32 v[198:199], v[202:203], v[156:157], v[198:199]\n" \
  "v_mfma_f32_32x32x8bf16_1k v[144:159], v[214:215], v[216:217], 0\n" \
  "s_waitcnt lgkmcnt(3)\n" \
  "ds_read_b32 v237, v250 offset:32\n" \
  "v_rcp_f32 v192, v192\n" \
  "v_add_f32 v222, v222, v223\n" \
  "v_rcp_f32 v193, v193\n" \
  "v_mfma_f32_32x32x16_bf16 v[144:159], v[24:27], v[160:163], v[144:159]\n" \
  "s_waitcnt lgkmcnt(3)\n" \
  "ds_read_b128 v[160:163], v228 offset:26112\n" \
  "v_rcp_f32 v194, v194\n" \
  "v_mul_f32 v222, v222, v218\n" \
  "v_rcp_f32 v195, v195\n" \
  "v_add_u32_e64 v226, s46, v226\n" \
  "v_mfma_f32_32x32x16_bf16 v[144:159], v[28:31], v[164:167], v[144:159]\n" \
  "s_waitcnt lgkmcnt(3)\n" \
  "ds_read_b128 v[164:167], v229 offset:26112\n" \
  "v_exp_f32 v128, v128\n" \
  "v_cvt_pk_bf16_f32 v239, v222, v222\n" \
  "v_exp_f32 v129, v129\n" \
  "v_add_u32 v227, s46, v227\n" \
  "v_mfma_f32_32x32x16_bf16 v[144:159], v[56:59], v[168:171], v[144:159]\n" \
  "s_waitcnt lgkmcnt(2)\n" \
  "ds_read_b128 v[168:171], v230 offset:26112\n" \
  "v_cmp_gt_u32 vcc, 4, v237\n" \
  "v_exp_f32 v130, v130\n" \
  "v_exp_f32 v131, v131\n" \
  "v_mfma_f32_32x32x16_bf16 v[144:159], v[60:63], v[172:175], v[144:159]\n" \
  "s_waitcnt lgkmcnt(2)\n" \
  "ds_read_b128 v[172:175], v231 offset:26112\n" \
  "v_exp_f32 v132, v132\n" \
  "v_exp_f32 v133, v133\n" \
  "s_cbranch_vccnz Lsp3%=\n" \
  "Lgo3%=:\n" \
  "v_mfma_f32_32x32x16_bf16 v[144:159], v[88:91], v[160:163], v[144:159]\n" \
  "s_waitcnt lgkmcnt(2)\n" \
  "ds_read_b128 v[160:163], v224 offset:0\n" \
  "ds_write_b16 v252, v239 offset:0\n" \
  "v_exp_f32 v134, v134\n" \
  "v_exp_f32 v135, v135\n" \
  "v_mov_b32 v233, v252\n" \
  "v_mfma_f32_32x32x16_bf16 v[144:159], v[92:95], v[164:167], v[144:159]\n" \
  "s_waitcnt lgkmcnt(3)\n" \
  "ds_read_b128 v[164:167], v225 offset:0\n" \
  "v_exp_f32 v136, v136\n" \
  "v_exp_f32 v137, v137\n" \
  "v_add_u32 v228, s46, v228\n" \
  "v_add_u32_e64 v230, s46, v230\n" \
  "v_mfma_f32_32x32x16_bf16 v[144:159], v[120:123], v[168:171], v[144:159]\n" \
  "s_waitcnt lgkmcnt(3)\n" \
  "ds_read_b128 v[168:171], v226 offset:0\n" \
  "v_exp_f32 v138, v138\n" \
  "v_exp_f32 v139, v139\n" \
  "v_add_u32 v229, s46, v229\n" \
  "v_add_u32_e64 v231, s46, v231\n" \
  "v_mfma_f32_32x32x16_bf16 v[144:159], v[124:127], v[172:175], v[144:159]\n" \
  "ds_read_b128 v[172:175], v227 offset:0\n" \
  "v_exp_f32 v140, v140\n" \
  "v_exp_f32 v141, v141\n" \
  "v_exp_f32 v142, v142\n" \
  "v_exp_f32 v143, v143\n" \
  "v_add_u32_e64 v252, s46, v252\n" \
  "s_cmp_lg_u32 s44, s45\n" \
  "s_cbranch_scc1 Lph0%=\n" \
  "v_pk_add_f32 v[128:129], v[128:129], v[176:177] clamp\n" \
  "v_pk_fma_f32 v[222:223], v[196:197], v[192:193], v[204:205]\n" \
  "v_pk_add_f32 v[130:131], v[130:131], v[178:179] clamp\n" \
  "v_pk_add_f32 v[136:137], v[136:137], v[184:185] clamp\n" \
  "v_pk_fma_f32 v[222:223], v[198:199], v[194:195], v[222:223]\n" \
  "v_pk_add_f32 v[138:139], v[138:139], v[186:187] clamp\n" \
  "v_pk_add_f32 v[132:133], v[132:133], v[180:181] clamp\n" \
  "v_pk_add_f32 v[134:135], v[134:135], v[182:183] clamp\n" \
  "v_pk_add_f32 v[140:141], v[140:141], v[188:189] clamp\n" \
  "v_pk_add_f32 v[142:143], v[142:143], v[190:191] clamp\n" \
  "v_pk_add_f32 v[200:201], v[128:129], v[130:131]\n" \
  "v_pk_add_f32 v[202:203], v[136:137], v[138:139]\n" \
  "v_pk_mul_f32 v[128:129], v[128:129], v[130:131]\n" \
  "v_pk_mul_f32 v[136:137], v[136:137], v[138:139]\n" \
  "v_pk_add_f32 v[130:131], v[132:133], v[134:135]\n" \
  "v_pk_add_f32 v[138:139], v[140:141], v[142:143]\n" \
  "v_pk_mul_f32 v[132:133], v[132:133], v[134:135]\n" \
  "v_pk_mul_f32 v[140:141], v[140:141], v[142:143]\n" \
  "v_pk_mul_f32 v[196:197], v[130:131], v[128:129]\n" \
  "v_pk_mul_f32 v[198:199], v[138:139], v[136:137]\n" \
  "v_pk_mul_f32 v[192:193], v[128:129], v[132:133]\n" \
  "v_pk_mul_f32 v[194:195], v[136:137], v[140:141]\n" \
  "v_pk_fma_f32 v[196:197], v[200:201], v[132:133], v[196:197]\n" \
  "v_pk_fma_f32 v[198:199], v[202:203], v[140:141], v[198:199]\n" \
  "v_rcp_f32 v192, v192\n" \
  "v_rcp_f32 v193, v193\n" \
  "v_rcp_f32 v194, v194\n" \
  "v_rcp_f32 v195, v195\n" \
  "v_add_f32 v222, v222, v223\n" \
  "v_mul_f32 v222, v222, v219\n" \
  "v_cvt_pk_bf16_f32 v239, v222, v222\n" \
  "ds_write_b16 v233, v239 offset:8704\n" \
  "s_mov_b64 exec, 3\n" \
  "ds_inc_u32 v254, v254 offset:16\n" \
  "s_mov_b64 exec, -1\n" \
  "v_exp_f32 v144, v144\n" \
  "v_exp_f32 v145, v145\n" \
  "v_exp_f32 v146, v146\n" \
  "v_exp_f32 v147, v147\n" \
  "v_exp_f32 v148, v148\n" \
  "v_exp_f32 v149, v149\n" \
  "v_exp_f32 v150, v150\n" \
  "v_exp_f32 v151, v151\n" \
  "v_exp_f32 v152, v152\n" \
  "v_exp_f32 v153, v153\n" \
  "v_exp_f32 v154, v154\n" \
  "v_exp_f32 v155, v155\n" \
  "v_exp_f32 v156, v156\n" \
  "v_exp_f32 v157, v157\n" \
  "v_exp_f32 v158, v158\n" \
  "v_exp_f32 v159, v159\n" \
  "v_pk_add_f32 v[144:145], v[144:145], v[176:177] clamp\n" \
  "v_pk_fma_f32 v[222:223], v[196:197], v[192:193], v[204:205]\n" \
  "v_pk_add_f32 v[146:147], v[146:147], v[178:179] clamp\n" \
  "v_pk_add_f32 v[152:153], v[152:153], v[184:185] clamp\n" \
  "v_pk_fma_f32 v[222:223], v[198:199], v[194:195], v[222:223]\n" \
  "v_pk_add_f32 v[154:155], v[154:155], v[186:187] clamp\n" \
  "v_pk_add_f32 v[148:149], v[148:149], v[180:181] clamp\n" \
  "v_pk_add_f32 v[150:151], v[150:151], v[182:183] clamp\n" \
  "v_pk_add_f32 v[156:157], v[156:157], v[188:189] clamp\n" \
  "v_pk_add_f32 v[158:159], v[158:159], v[190:191] clamp\n" \
  "v_pk_add_f32 v[200:201], v[144:145], v[146:147]\n" \
  "v_pk_add_f32 v[202:203], v[152:153], v[154:155]\n" \
  "v_pk_mul_f32 v[144:145], v[144:145], v[146:147]\n" \
  "v_pk_mul_f32 v[152:153], v[152:153], v[154:155]\n" \
  "v_pk_add_f32 v[146:147], v[148:149], v[150:151]\n" \
  "v_pk_add_f32 v[154:155], v[156:157], v[158:159]\n" \
  "v_pk_mul_f32 v[148:149], v[148:149], v[150:151]\n" \
  "v_pk_mul_f32 v[156:157], v[156:157], v[158:159]\n" \
  "v_pk_mul_f32 v[196:197], v[146:147], v[144:145]\n" \
  "v_pk_mul_f32 v[198:199], v[154:155], v[152:153]\n" \
  "v_pk_mul_f32 v[192:193], v[144:145], v[148:149]\n" \
  "v_pk_mul_f32 v[194:195], v[152:153], v[156:157]\n" \
  "v_pk_fma_f32 v[196:197], v[200:201], v[148:149], v[196:197]\n" \
  "v_pk_fma_f32 v[198:199], v[202:203], v[156:157], v[198:199]\n" \
  "v_rcp_f32 v192, v192\n" \
  "v_rcp_f32 v193, v193\n" \
  "v_rcp_f32 v194, v194\n" \
  "v_rcp_f32 v195, v195\n" \
  "v_add_f32 v222, v222, v223\n" \
  "v_mul_f32 v222, v222, v220\n" \
  "v_cvt_pk_bf16_f32 v239, v222, v222\n" \
  "ds_write_b16 v233, v239 offset:17408\n" \
  "v_pk_fma_f32 v[222:223], v[196:197], v[192:193], v[204:205]\n" \
  "v_pk_fma_f32 v[222:223], v[198:199], v[194:195], v[222:223]\n" \
  "v_add_f32 v222, v222, v223\n" \
  "v_mul_f32 v222, v222, v221\n" \
  "v_cvt_pk_bf16_f32 v239, v222, v222\n" \
  "ds_write_b16 v233, v239 offset:26112\n" \
  "s_mov_b64 exec, 12\n" \
  "ds_inc_u32 v254, v254 offset:16\n" \
  "s_mov_b64 exec, -1\n" \
  "s_branch Lend%=\n" \
  "Lsp0%=:\n" \
  "s_mov_b32 s47, 0\n" \
  "Lspl0%=:\n" \
  "ds_read_b32 v237, v250 offset:36\n" \
  "s_add_u32 s47, s47, 1\n" \
  "s_waitcnt lgkmcnt(0)\n" \
  "v_readfirstlane_b32 s48, v237\n" \
  "s_cmp_ge_u32 s48, 4\n" \
  "s_cbranch_scc1 Lgo0%=\n" \
  "s_cmp_lt_u32 s47, 0x40000\n" \
  "s_cbranch_scc1 Lspl0%=\n" \
  "s_branch Lgo0%=\n" \
  "Lsp1%=:\n" \
  "s_mov_b32 s47, 0\n" \
  "Lspl1%=:\n" \
  "ds_read_b32 v237, v250 offset:40\n" \
  "s_add_u32 s47, s47, 1\n" \
  "s_waitcnt lgkmcnt(0)\n" \
  "v_readfirstlane_b32 s48, v237\n" \
  "s_cmp_ge_u32 s48, 4\n" \
  "s_cbranch_scc1 Lgo1%=\n" \
  "s_cmp_lt_u32 s47, 0x40000\n" \
  "s_cbranch_scc1 Lspl1%=\n" \
  "s_branch Lgo1%=\n" \
  "Lsp2%=:\n" \
  "s_mov_b32 s47, 0\n" \
  "Lspl2%=:\n" \
  "ds_read_b32 v237, v250 offset:28\n" \
  "s_add_u32 s47, s47, 1\n" \
  "s_waitcnt lgkmcnt(0)\n" \
  "v_readfirstlane_b32 s48, v237\n" \
  "s_cmp_ge_u32 s48, 4\n" \
  "s_cbranch_scc1 Lgo2%=\n" \
  "s_cmp_lt_u32 s47, 0x40000\n" \
  "s_cbranch_scc1 Lspl2%=\n" \
  "s_branch Lgo2%=\n" \
  "Lsp3%=:\n" \
  "s_mov_b32 s47, 0\n" \
  "Lspl3%=:\n" \
  "ds_read_b32 v237, v250 offset:32\n" \
  "s_add_u32 s47, s47, 1\n" \
  "s_waitcnt lgkmcnt(0)\n" \
  "v_readfirstlane_b32 s48, v237\n" \
  "s_cmp_ge_u32 s48, 4\n" \
  "s_cbranch_scc1 Lgo3%=\n" \
  "s_cmp_lt_u32 s47, 0x40000\n" \
  "s_cbranch_scc1 Lspl3%=\n" \
  "s_branch Lgo3%=\n" \
  "Lspp%=:\n" \
  "s_mov_b32 s47, 0\n" \
  "Lsppl%=:\n" \
  "ds_read_b32 v237, v250 offset:36\n" \
  "s_add_u32 s47, s47, 1\n" \
  "s_waitcnt lgkmcnt(0)\n" \
  "v_readfirstlane_b32 s48, v237\n" \
  "s_cmp_ge_u32 s48, 4\n" \
  "s_cbranch_scc1 Lgop%=\n" \
  "s_cmp_lt_u32 s47, 0x40000\n" \
  "s_cbranch_scc1 Lsppl%=\n" \
  "s_branch Lgop%=\n" \
  "Lw3%=:\n" \
  "s_mov_b32 s47, 0\n" \
  "Lw3l%=:\n" \
  "ds_read_b32 v183, v255 offset:8\n" \
  "s_add_u32 s47, s47, 1\n" \
  "s_waitcnt lgkmcnt(0)\n" \
  "v_readfirstlane_b32 s48, v183\n" \
  "s_cmp_ge_u32 s48, 4\n" \
  "s_cbranch_scc1 Lw3d%=\n" \
  "s_cmp_lt_u32 s47, 0x40000\n" \
  "s_cbranch_scc1 Lw3l%=\n" \
  "s_branch Lw3d%=\n" \
  "Lcr%=:\n" \
  "v_readfirstlane_b32 s48, v208\n" \
  "s_add_u32 s47, s47, 1\n" \
  "s_cmp_lt_u32 s47, 0x40000\n" \
  "s_cbranch_scc0 Lend%=\n" \
  "s_cmp_ge_u32 s48, 4\n" \
  "s_cselect_b32 s48, s45, 1\n" \
  "s_cmp_eq_u32 s48, 0\n" \
  "s_cbranch_scc1 Lend%=\n" \
  "ds_read_b32 v208, v250 offset:416\n" \
  "ds_read_b32 v209, v250 offset:32\n" \
  "ds_read_b32 v210, v255 offset:4\n" \
  "ds_read_b128 v[232:235], v250 offset:432\n" \
  "ds_read_b128 v[212:215], v248\n" \
  "s_branch Lchk%=\n" \
  "Lend%=:\n" \
  "s_waitcnt lgkmcnt(0)\n"
#define COMPUTE_CLOB \
  "v0", "v1", "v2", "v3", "v4", "v5", "v6", "v7", "v8", "v9", "v10", "v11", "v12", "v13", "v14", "v15", "v16", "v17", "v18", "v19", "v20", "v21", "v22", "v23", \
  "v24", "v25", "v26", "v27", "v28", "v29", "v30", "v31", "v32", "v33", "v34", "v35", "v36", "v37", "v38", "v39", "v40", "v41", "v42", "v43", "v44", "v45", "v46", "v47", \
  "v48", "v49", "v50", "v51", "v52", "v53", "v54", "v55", "v56", "v57", "v58", "v59", "v60", "v61", "v62", "v63", "v64", "v65", "v66", "v67", "v68", "v69", "v70", "v71", \
  "v72", "v73", "v74", "v75", "v76", "v77", "v78", "v79", "v80", "v81", "v82", "v83", "v84", "v85", "v86", "v87", "v88", "v89", "v90", "v91", "v92", "v93", "v94", "v95", \
  "v96", "v97", "v98", "v99", "v100", "v101", "v102", "v103", "v104", "v105", "v106", "v107", "v108", "v109", "v110", "v111", "v112", "v113", "v114", "v115", "v116", "v117", "v118", "v119", \
  "v120", "v121", "v122", "v123", "v124", "v125", "v126", "v127", "v128", "v129", "v130", "v131", "v132", "v133", "v134", "v135", "v136", "v137", "v138", "v139", "v140", "v141", "v142", "v143", \
  "v144", "v145", "v146", "v147", "v148", "v149", "v150", "v151", "v152", "v153", "v154", "v155", "v156", "v157", "v158", "v159", "v160", "v161", "v162", "v163", "v164", "v165", "v166", "v167", \
  "v168", "v169", "v170", "v171", "v172", "v173", "v174", "v175", "v192", "v193", "v194", "v195", "v196", "v197", "v198", "v199", "v200", "v201", "v202", "v203", "v204", "v205", "v206", "v207", \
  "v208", "v209", "v210", "v211", "v212", "v213", "v214", "v215", "v216", "v217", "v218", "v219", "v220", "v221", "v222", "v223", "v232", "v233", "v234", "v235", "v236", "v237", "v238", "v239", \
  "s40", "s41", "s42", "s43", "s44", "s45", "s46", "s47", "s48", "s49", "s50", "s51", "s52", "s53", "vcc", "scc", "memory"
#define HELPER_ASM \
  "s_load_dword s65, %[b2p], 0x0\n" \
  "s_mul_i32 s61, %[q], 8704\n" \
  "s_lshl_b32 s60, %[q], 6\n" \
  "s_add_u32 s66, %[w1lo], s60\n" \
  "s_addc_u32 s67, %[w1hi], 0\n" \
  "s_lshl_b32 s57, %[q], 4\n" \
  "s_add_u32 s57, s57, 146176\n" \
  "v_mov_b32 v15, s57\n" \
  "s_lshl_b32 s57, %[q], 2\n" \
  "s_add_u32 s57, s57, 146176\n" \
  "v_mov_b32 v125, s57\n" \
  "v_mov_b32 v122, 1\n" \
  "v_mov_b32 v123, 4\n" \
  "v_mov_b32 v121, 146176\n" \
  "v_mov_b32 v126, 146176\n" \
  "v_mov_b32 v120, 146188\n" \
  "s_add_u32 s68, %[hlo], 0\n" \
  "s_addc_u32 s69, %[hhi], 0\n" \
  "s_mov_b32 s70, 51200\n" \
  "s_mov_b32 s71, 0x20000\n" \
  "s_add_u32 s72, %[hlo], 51200\n" \
  "s_addc_u32 s73, %[hhi], 0\n" \
  "s_mov_b32 s74, 51200\n" \
  "s_mov_b32 s75, 0x20000\n" \
  "s_add_u32 s76, %[hlo], 102400\n" \
  "s_addc_u32 s77, %[hhi], 0\n" \
  "s_mov_b32 s78, 51200\n" \
  "s_mov_b32 s79, 0x20000\n" \
  "s_add_u32 s80, %[hlo], 153600\n" \
  "s_addc_u32 s81, %[hhi], 0\n" \
  "s_mov_b32 s82, 51200\n" \
  "s_mov_b32 s83, 0x20000\n" \
  "v_mov_b32 v64, 0\n" \
  "v_mov_b32 v65, 0\n" \
  "v_mov_b32 v66, 0\n" \
  "v_mov_b32 v67, 0\n" \
  "v_mov_b32 v68, 0\n" \
  "v_mov_b32 v69, 0\n" \
  "v_mov_b32 v70, 0\n" \
  "v_mov_b32 v71, 0\n" \
  "v_mov_b32 v72, 0\n" \
  "v_mov_b32 v73, 0\n" \
  "v_mov_b32 v74, 0\n" \
  "v_mov_b32 v75, 0\n" \
  "v_mov_b32 v76, 0\n" \
  "v_mov_b32 v77, 0\n" \
  "v_mov_b32 v78, 0\n" \
  "v_mov_b32 v79, 0\n" \
  "v_mov_b32 v80, 0\n" \
  "v_mov_b32 v81, 0\n" \
  "v_mov_b32 v82, 0\n" \
  "v_mov_b32 v83, 0\n" \
  "v_mov_b32 v84, 0\n" \
  "v_mov_b32 v85, 0\n" \
  "v_mov_b32 v86, 0\n" \
  "v_mov_b32 v87, 0\n" \
  "v_mov_b32 v88, 0\n" \
  "v_mov_b32 v89, 0\n" \
  "v_mov_b32 v90, 0\n" \
  "v_mov_b32 v91, 0\n" \
  "v_mov_b32 v92, 0\n" \
  "v_mov_b32 v93, 0\n" \
  "v_mov_b32 v94, 0\n" \
  "v_mov_b32 v95, 0\n" \
  "s_waitcnt vmcnt(0)\n" \
  "s_setprio 3\n" \
  "ds_read_b32 v127, v121 offset:400\n" \
  "v_cmp_neq_f16_sdwa s[40:41], v248, v39 src0_sel:WORD_0 src1_sel:WORD_0\n" \
  "v_cmp_neq_f16_sdwa s[42:43], v248, v39 src0_sel:WORD_1 src1_sel:WORD_0\n" \
  "v_cmp_neq_f16_sdwa s[44:45], v249, v39 src0_sel:WORD_0 src1_sel:WORD_0\n" \
  "v_cmp_neq_f16_sdwa s[46:47], v249, v39 src0_sel:WORD_1 src1_sel:WORD_0\n" \
  "s_bcnt1_i32_b64 s48, s[40:41]\n" \
  "s_bcnt1_i32_b64 s49, s[42:43]\n" \
  "s_bcnt1_i32_b64 s50, s[44:45]\n" \
  "s_bcnt1_i32_b64 s51, s[46:47]\n" \
  "s_add_u32 s48, s48, s49\n" \
  "s_add_u32 s50, s50, s51\n" \
  "s_add_u32 s48, s48, s50\n" \
  "s_sub_u32 s49, 0, s48\n" \
  "s_and_b32 s49, s49, 31\n" \
  "s_lshr_b32 s50, 15, %[q]\n" \
  "s_bitcmp1_b32 s50, 0\n" \
  "s_cselect_b32 s49, s49, 0\n" \
  "v_mbcnt_lo_u32_b32 v112, s40, 0\n" \
  "v_mbcnt_hi_u32_b32 v112, s41, v112\n" \
  "v_mbcnt_lo_u32_b32 v113, s44, 0\n" \
  "v_mbcnt_lo_u32_b32 v112, s42, v112\n" \
  "v_mbcnt_hi_u32_b32 v113, s45, v113\n" \
  "v_mbcnt_hi_u32_b32 v112, s43, v112\n" \
  "v_mbcnt_lo_u32_b32 v113, s46, v113\n" \
  "v_mbcnt_hi_u32_b32 v113, s47, v113\n" \
  "s_waitcnt lgkmcnt(0)\n" \
  "v_cmp_gt_u32 vcc, 4, v127\n" \
  "s_nop 4\n" \
  "s_cbranch_vccz Hw0%=d\n" \
  "s_mov_b32 s56, 0\n" \
  "Hw0%=:\n" \
  "ds_read_b32 v124, v121 offset:400\n" \
  "s_add_u32 s56, s56, 1\n" \
  "s_waitcnt lgkmcnt(0)\n" \
  "v_readfirstlane_b32 s57, v124\n" \
  "s_cmp_ge_u32 s57, 4\n" \
  "s_cbranch_scc1 Hw0%=d\n" \
  "s_cmp_lt_u32 s56, 0x40000\n" \
  "s_cbranch_scc1 Hw0%=\n" \
  "Hw0%=d:\n" \
  "s_add_u32 m0, s61, 69632\n" \
  "s_add_u32 s62, s66, 24576\n" \
  "s_addc_u32 s63, s67, 0\n" \
  "global_load_lds_dwordx4 v36, s[62:63]\n" \
  "v_add3_u32 v2, v112, v113, s49\n" \
  "v_addc_co_u32_e64 v3, s[58:59], v2, 0, s[40:41]\n" \
  "s_add_u32 m0, s61, 70656\n" \
  "s_add_u32 s62, s66, 28672\n" \
  "s_addc_u32 s63, s67, 0\n" \
  "global_load_lds_dwordx4 v36, s[62:63]\n" \
  "v_addc_co_u32_e64 v4, s[58:59], v3, 0, s[42:43]\n" \
  "v_addc_co_u32_e64 v5, s[58:59], v4, 0, s[44:45]\n" \
  "v_lshl_add_u32 v6, v2, 1, v34\n" \
  "s_add_u32 m0, s61, 71680\n" \
  "s_add_u32 s62, s66, 90112\n" \
  "s_addc_u32 s63, s67, 0\n" \
  "global_load_lds_dwordx4 v36, s[62:63]\n" \
  "v_lshl_add_u32 v7, v3, 1, v34\n" \
  "v_lshl_add_u32 v8, v4, 1, v34\n" \
  "v_lshl_add_u32 v9, v5, 1, v34\n" \
  "s_add_u32 m0, s61, 72704\n" \
  "s_add_u32 s62, s66, 94208\n" \
  "s_addc_u32 s63, s67, 0\n" \
  "global_load_lds_dwordx4 v36, s[62:63]\n" \
  "s_mov_b64 exec, s[40:41]\n" \
  "ds_write_b16 v6, v16\n" \
  "s_mov_b64 exec, s[42:43]\n" \
  "ds_write_b16 v7, v17\n" \
  "s_mov_b64 exec, s[44:45]\n" \
  "ds_write_b16 v8, v18\n" \
  "s_mov_b64 exec, s[46:47]\n" \
  "ds_write_b16 v9, v19\n" \
  "s_mov_b64 exec, -1\n" \
  "v_lshl_add_u32 v6, v2, 4, v35\n" \
  "v_lshl_add_u32 v7, v3, 4, v35\n" \
  "s_add_u32 m0, s61, 73728\n" \
  "s_add_u32 s62, s66, 122880\n" \
  "s_addc_u32 s63, s67, 0\n" \
  "global_load_lds_dwordx4 v36, s[62:63]\n" \
  "v_lshl_add_u32 v8, v4, 4, v35\n" \
  "v_lshl_add_u32 v9, v5, 4, v35\n" \
  "s_add_u32 s48, s48, 31\n" \
  "s_add_u32 m0, s61, 74752\n" \
  "s_add_u32 s62, s66, 126976\n" \
  "s_addc_u32 s63, s67, 0\n" \
  "global_load_lds_dwordx4 v36, s[62:63]\n" \
  "s_lshr_b32 s48, s48, 5\n" \
  "v_mov_b32 v14, s48\n" \
  "s_mov_b64 exec, s[40:41]\n" \
  "ds_write_b16 v6, v248 offset:2\n" \
  "s_mov_b64 exec, s[42:43]\n" \
  "ds_write_b16_d16_hi v7, v248 offset:2\n" \
  "s_mov_b64 exec, s[44:45]\n" \
  "ds_write_b16 v8, v249 offset:2\n" \
  "s_mov_b64 exec, s[46:47]\n" \
  "ds_write_b16_d16_hi v9, v249 offset:2\n" \
  "s_mov_b64 exec, 1\n" \
  "ds_write_b32 v125, v14 offset:432\n" \
  "s_mov_b64 exec, -1\n" \
  "s_add_u32 m0, s61, 104448\n" \
  "s_add_u32 s62, s66, 32768\n" \
  "s_addc_u32 s63, s67, 0\n" \
  "global_load_lds_dwordx4 v36, s[62:63]\n" \
  "s_add_u32 m0, s61, 105472\n" \
  "s_add_u32 s62, s66, 36864\n" \
  "s_addc_u32 s63, s67, 0\n" \
  "global_load_lds_dwordx4 v36, s[62:63]\n" \
  "s_add_u32 m0, s61, 106496\n" \
  "s_add_u32 s62, s66, 40960\n" \
  "s_addc_u32 s63, s67, 0\n" \
  "global_load_lds_dwordx4 v36, s[62:63]\n" \
  "s_add_u32 m0, s61, 107520\n" \
  "s_add_u32 s62, s66, 45056\n" \
  "s_addc_u32 s63, s67, 0\n" \
  "global_load_lds_dwordx4 v36, s[62:63]\n" \
  "s_add_u32 m0, s61, 108544\n" \
  "s_add_u32 s62, s66, 49152\n" \
  "s_addc_u32 s63, s67, 0\n" \
  "global_load_lds_dwordx4 v36, s[62:63]\n" \
  "s_add_u32 m0, s61, 109568\n" \
  "s_add_u32 s62, s66, 53248\n" \
  "s_addc_u32 s63, s67, 0\n" \
  "global_load_lds_dwordx4 v36, s[62:63]\n" \
  "s_add_u32 m0, s61, 110592\n" \
  "s_add_u32 s62, s66, 57344\n" \
  "s_addc_u32 s63, s67, 0\n" \
  "global_load_lds_dwordx4 v36, s[62:63]\n" \
  "s_add_u32 m0, s61, 111616\n" \
  "s_add_u32 s62, s66, 61440\n" \
  "s_addc_u32 s63, s67, 0\n" \
  "global_load_lds_dwordx4 v36, s[62:63]\n" \
  "s_mov_b64 exec, 0xffff\n" \
  "s_mov_b32 m0, 145664\n" \
  "s_nop 0\n" \
  "global_load_lds_dwordx4 v24, %[b1p]\n" \
  "s_mov_b64 exec, -1\n" \
  "s_mov_b64 exec, 1\n" \
  "ds_add_u32 v121, v122 offset:416\n" \
  "s_mov_b64 exec, -1\n" \
  "s_mov_b32 s56, 0\n" \
  "Hcr%=:\n" \
  "ds_read_b32 v124, v121 offset:416\n" \
  "ds_read_b128 v[112:115], v121 offset:432\n" \
  "ds_read_b32 v108, v25 offset:0\n" \
  "ds_read_b32 v109, v25 offset:448\n" \
  "ds_read_b32 v110, v25 offset:896\n" \
  "ds_read_b32 v111, v25 offset:1344\n" \
  "s_add_u32 s56, s56, 1\n" \
  "s_waitcnt lgkmcnt(0)\n" \
  "v_readfirstlane_b32 s57, v124\n" \
  "s_cmp_ge_u32 s57, 4\n" \
  "s_cbranch_scc1 Hcr%=d\n" \
  "s_cmp_lt_u32 s56, 0x40000\n" \
  "s_cbranch_scc1 Hcr%=\n" \
  "Hcr%=d:\n" \
  "v_max_u32 v112, v112, v113\n" \
  "v_max3_u32 v114, v112, v114, v115\n" \
  "s_lshl_b32 s61, %[q], 11\n" \
  "v_add_u32_sdwa v112, v26, v108 dst_sel:DWORD dst_unused:UNUSED_PAD src0_sel:DWORD src1_sel:WORD_0\n" \
  "v_add_u32_sdwa v113, v27, v108 dst_sel:DWORD dst_unused:UNUSED_PAD src0_sel:DWORD src1_sel:WORD_1\n" \
  "s_add_u32 m0, s61, 0\n" \
  "ds_read_b32 v108, v25 offset:64\n" \
  "buffer_load_dwordx4 v112, s[68:71], 0 offen nt lds\n" \
  "s_add_u32 m0, s61, 1024\n" \
  "s_nop 0\n" \
  "buffer_load_dwordx4 v113, s[68:71], 0 offen nt lds\n" \
  "v_readfirstlane_b32 s52, v114\n" \
  "v_lshl_add_u32 v116, v114, 4, v121\n" \
  "s_mov_b32 s54, 0\n" \
  "s_mov_b32 s53, 0\n" \
  "s_waitcnt vmcnt(11)\n" \
  "s_mov_b64 exec, 1\n" \
  "ds_add_u32 v15, v123 offset:456\n" \
  "s_mov_b64 exec, -1\n" \
  "s_cmp_eq_u32 s52, 0\n" \
  "s_cbranch_scc1 Hout%=\n" \
  "v_add_u32_sdwa v112, v26, v109 dst_sel:DWORD dst_unused:UNUSED_PAD src0_sel:DWORD src1_sel:WORD_0\n" \
  "v_add_u32_sdwa v113, v27, v109 dst_sel:DWORD dst_unused:UNUSED_PAD src0_sel:DWORD src1_sel:WORD_1\n" \
  "s_add_u32 m0, s61, 8704\n" \
  "ds_read_b32 v109, v25 offset:512\n" \
  "buffer_load_dwordx4 v112, s[72:75], 0 offen nt lds\n" \
  "s_add_u32 m0, s61, 9728\n" \
  "s_nop 0\n" \
  "buffer_load_dwordx4 v113, s[72:75], 0 offen nt lds\n" \
  "ds_read_b32 v127, v121 offset:404\n" \
  "v_add_u32_sdwa v112, v26, v110 dst_sel:DWORD dst_unused:UNUSED_PAD src0_sel:DWORD src1_sel:WORD_0\n" \
  "v_add_u32_sdwa v113, v27, v110 dst_sel:DWORD dst_unused:UNUSED_PAD src0_sel:DWORD src1_sel:WORD_1\n" \
  "s_add_u32 m0, s61, 17408\n" \
  "ds_read_b32 v110, v25 offset:960\n" \
  "buffer_load_dwordx4 v112, s[76:79], 0 offen nt lds\n" \
  "s_add_u32 m0, s61, 18432\n" \
  "s_nop 0\n" \
  "buffer_load_dwordx4 v113, s[76:79], 0 offen nt lds\n" \
  "v_add_u32_sdwa v112, v26, v111 dst_sel:DWORD dst_unused:UNUSED_PAD src0_sel:DWORD src1_sel:WORD_0\n" \
  "v_add_u32_sdwa v113, v27, v111 dst_sel:DWORD dst_unused:UNUSED_PAD src0_sel:DWORD src1_sel:WORD_1\n" \
  "s_add_u32 m0, s61, 26112\n" \
  "ds_read_b32 v111, v25 offset:1408\n" \
  "buffer_load_dwordx4 v112, s[80:83], 0 offen nt lds\n" \
  "s_add_u32 m0, s61, 27136\n" \
  "s_nop 0\n" \
  "buffer_load_dwordx4 v113, s[80:83], 0 offen nt lds\n" \
  "s_mov_b64 exec, 1\n" \
  "ds_add_u32 v116, v122 offset:32\n" \
  "s_mov_b64 exec, -1\n" \
  "s_waitcnt vmcnt(8)\n" \
  "s_cmp_eq_u32 s52, 1\n" \
  "s_cbranch_scc1 Hcb%=\n" \
  "s_waitcnt lgkmcnt(0)\n" \
  "v_cmp_gt_u32 vcc, 4, v127\n" \
  "v_add_u32 v25, 64, v25\n" \
  "v_add_u32 v120, 16, v120\n" \
  "s_add_u32 s61, s61, 34816\n" \
  "s_mov_b32 s53, 1\n" \
  "s_nop 0\n" \
  "s_cbranch_vccz Hw1%=d\n" \
  "s_mov_b32 s56, 0\n" \
  "Hw1%=:\n" \
  "ds_read_b32 v124, v121 offset:404\n" \
  "s_add_u32 s56, s56, 1\n" \
  "s_waitcnt lgkmcnt(0)\n" \
  "v_readfirstlane_b32 s57, v124\n" \
  "s_cmp_ge_u32 s57, 4\n" \
  "s_cbranch_scc1 Hw1%=d\n" \
  "s_cmp_lt_u32 s56, 0x40000\n" \
  "s_cbranch_scc1 Hw1%=\n" \
  "Hw1%=d:\n" \
  "v_add_u32_sdwa v112, v26, v108 dst_sel:DWORD dst_unused:UNUSED_PAD src0_sel:DWORD src1_sel:WORD_0\n" \
  "v_add_u32_sdwa v113, v27, v108 dst_sel:DWORD dst_unused:UNUSED_PAD src0_sel:DWORD src1_sel:WORD_1\n" \
  "s_add_u32 m0, s61, 0\n" \
  "ds_read_b32 v108, v25 offset:64\n" \
  "buffer_load_dwordx4 v112, s[68:71], 0 offen nt lds\n" \
  "s_add_u32 m0, s61, 1024\n" \
  "s_nop 0\n" \
  "buffer_load_dwordx4 v113, s[68:71], 0 offen nt lds\n" \
  "v_add_u32_sdwa v112, v26, v109 dst_sel:DWORD dst_unused:UNUSED_PAD src0_sel:DWORD src1_sel:WORD_0\n" \
  "v_add_u32_sdwa v113, v27, v109 dst_sel:DWORD dst_unused:UNUSED_PAD src0_sel:DWORD src1_sel:WORD_1\n" \
  "s_add_u32 m0, s61, 8704\n" \
  "ds_read_b32 v109, v25 offset:512\n" \
  "buffer_load_dwordx4 v112, s[72:75], 0 offen nt lds\n" \
  "s_add_u32 m0, s61, 9728\n" \
  "s_nop 0\n" \
  "buffer_load_dwordx4 v113, s[72:75], 0 offen nt lds\n" \
  "Hcb%=:\n" \
  "ds_read_b32 v176, v40\n" \
  "ds_read_u16 v177, v41 offset:256\n" \
  "ds_read_u16 v182, v41\n" \
  "ds_read_b128 v[128:131], v37 offset:0\n" \
  "ds_read_b64_tr_b16 v[132:133], v20 offset:0\n" \
  "ds_read_b64_tr_b16 v[134:135], v22 offset:0\n" \
  "ds_read_b64_tr_b16 v[136:137], v21 offset:0\n" \
  "ds_read_b64_tr_b16 v[138:139], v23 offset:0\n" \
  "ds_read_b128 v[140:143], v37 offset:64\n" \
  "ds_read_b64_tr_b16 v[144:145], v20 offset:2048\n" \
  "ds_read_b64_tr_b16 v[146:147], v22 offset:2048\n" \
  "ds_read_b64_tr_b16 v[148:149], v21 offset:2048\n" \
  "ds_read_b64_tr_b16 v[150:151], v23 offset:2048\n" \
  "ds_read_b128 v[152:155], v37 offset:128\n" \
  "ds_read_b64_tr_b16 v[156:157], v20 offset:4096\n" \
  "ds_read_b64_tr_b16 v[158:159], v22 offset:4096\n" \
  "ds_read_b64_tr_b16 v[160:161], v21 offset:4096\n" \
  "ds_read_b64_tr_b16 v[162:163], v23 offset:4096\n" \
  "ds_read_b128 v[164:167], v37 offset:192\n" \
  "ds_read_b64_tr_b16 v[168:169], v20 offset:6144\n" \
  "ds_read_b64_tr_b16 v[170:171], v22 offset:6144\n" \
  "ds_read_b64_tr_b16 v[172:173], v21 offset:6144\n" \
  "ds_read_b64_tr_b16 v[174:175], v23 offset:6144\n" \
  "s_mov_b64 exec, 1\n" \
  "ds_add_u32 v121, v122 offset:412\n" \
  "s_mov_b64 exec, -1\n" \
  "s_waitcnt lgkmcnt(14)\n" \
  "v_mfma_f32_16x16x32_bf16 v[96:99], v[128:131], v[132:135], 0\n" \
  "v_mfma_f32_16x16x32_bf16 v[100:103], v[128:131], v[136:139], 0\n" \
  "s_waitcnt lgkmcnt(11)\n" \
  "v_mfma_f32_16x16x32_bf16 v[96:99], v[140:143], v[144:147], v[96:99]\n" \
  "v_mfma_f32_16x16x32_bf16 v[100:103], v[140:143], v[148:151], v[100:103]\n" \
  "s_waitcnt lgkmcnt(6)\n" \
  "v_mfma_f32_16x16x32_bf16 v[96:99], v[152:155], v[156:159], v[96:99]\n" \
  "v_mfma_f32_16x16x32_bf16 v[100:103], v[152:155], v[160:163], v[100:103]\n" \
  "s_waitcnt lgkmcnt(1)\n" \
  "v_mfma_f32_16x16x32_bf16 v[96:99], v[164:167], v[168:171], v[96:99]\n" \
  "v_mfma_f32_16x16x32_bf16 v[100:103], v[164:167], v[172:175], v[100:103]\n" \
  "v_and_b32 v178, 0x7fff0000, v176\n" \
  "v_lshlrev_b32 v176, 16, v176\n" \
  "v_and_b32 v176, 0x7fff0000, v176\n" \
  "v_max_u32 v176, v176, v178\n" \
  "v_lshlrev_b32 v177, 16, v177\n" \
  "v_min_f32 v179, 0, v177\n" \
  "s_nop 1\n" \
  "v_max_u32_dpp v176, v176, v176 row_shr:1\n" \
  "v_add_f32_dpp v179, v179, v179 quad_perm:[1,0,3,2]\n" \
  "s_nop 1\n" \
  "v_max_u32_dpp v176, v176, v176 row_shr:2\n" \
  "v_add_f32_dpp v179, v179, v179 quad_perm:[2,3,0,1]\n" \
  "s_nop 1\n" \
  "v_max_u32_dpp v176, v176, v176 row_shr:4\n" \
  "s_nop 1\n" \
  "v_max_u32_dpp v176, v176, v176 row_shr:8\n" \
  "v_add_f32_dpp v179, v179, v179 row_ror:8\n" \
  "s_nop 1\n" \
  "v_max_u32_dpp v176, v176, v176 row_bcast:15 row_mask:0xa\n" \
  "s_nop 1\n" \
  "v_max_u32_dpp v176, v176, v176 row_bcast:31 row_mask:0xc\n" \
  "v_mov_b32 v180, v179\n" \
  "v_readlane_b32 s64, v176, 63\n" \
  "v_lshlrev_b32 v182, 16, v182\n" \
  "v_permlane16_swap_b32 v179, v180\n" \
  "s_lshl_b32 s65, s65, 16\n" \
  "s_and_b32 s57, s65, 0x7fffffff\n" \
  "s_sub_i32 s57, s57, 0x08000000\n" \
  "s_max_i32 s64, s64, s57\n" \
  "s_and_b32 s64, s64, 0x7f800000\n" \
  "s_add_u32 s64, s64, 0x00800000\n" \
  "s_sub_u32 s57, 0x7f000000, s64\n" \
  "v_add_f32 v179, v179, v180\n" \
  "v_mul_f32_e64 v178, |v177|, s57\n" \
  "v_max_f32 v178, 0x30800000, v178\n" \
  "v_rcp_f32 v180, v178\n" \
  "v_log_f32 v178, v178\n" \
  "v_mov_b32 v181, 0x3e000000\n" \
  "v_fmac_f32 v179, s65, v181\n" \
  "v_mul_f32 v180, 0x30800000, v180\n" \
  "v_mul_f32 v178, 0x3f317218, v178\n" \
  "v_mul_f32 v179, s57, v179\n" \
  "v_and_b32 v177, 0x80000000, v177\n" \
  "v_mul_f32 v179, 0x4e800000, v179\n" \
  "v_xor_b32 v178, v178, v177\n" \
  "ds_write_b32 v42, v180\n" \
  "ds_write_b32 v43, v179\n" \
  "v_add_f32 v178, v178, v182\n" \
  "s_mov_b32 s62, 0xffff0000\n" \
  "s_mov_b32 s63, 0xffff0000\n" \
  "v_cndmask_b32 v96, v96, v100, s[62:63]\n" \
  "v_cndmask_b32 v97, v97, v101, s[62:63]\n" \
  "v_cndmask_b32 v98, v98, v102, s[62:63]\n" \
  "v_cndmask_b32 v99, v99, v103, s[62:63]\n" \
  "v_add_f32 v96, v96, v178\n" \
  "v_add_f32 v97, v97, v178\n" \
  "v_add_f32 v98, v98, v178\n" \
  "v_add_f32 v99, v99, v178\n" \
  "ds_write_b128 v38, v[96:99]\n" \
  "s_mov_b64 exec, 1\n" \
  "ds_add_u32 v15, v123 offset:452\n" \
  "s_mov_b64 exec, -1\n" \
  "s_cmp_eq_u32 s52, 1\n" \
  "s_cbranch_scc1 Hend%=\n" \
  "s_setprio 0\n" \
  "s_branch Hmid%=\n" \
  "Hloop%=:\n" \
  "s_cmp_ge_u32 s53, s52\n" \
  "s_cbranch_scc1 Hlad%=\n" \
  "v_add_u32 v25, 64, v25\n" \
  "s_and_b32 s57, s53, 3\n" \
  "s_mul_i32 s61, s57, 34816\n" \
  "s_lshl_b32 s57, %[q], 11\n" \
  "s_add_u32 s61, s61, s57\n" \
  "s_cmp_ge_u32 s53, 4\n" \
  "s_cbranch_scc1 Hrec%=\n" \
  "s_lshl_b32 s57, s53, 2\n" \
  "s_add_u32 s57, s57, 146576\n" \
  "Hgo%=:\n" \
  "v_mov_b32 v114, s57\n" \
  "s_mov_b32 s56, 0\n" \
  "Hw2%=:\n" \
  "ds_read_b32 v124, v114 offset:0\n" \
  "s_add_u32 s56, s56, 1\n" \
  "s_waitcnt lgkmcnt(0)\n" \
  "v_readfirstlane_b32 s57, v124\n" \
  "s_cmp_ge_u32 s57, 4\n" \
  "s_cbranch_scc1 Hw2%=d\n" \
  "s_sleep 1\n" \
  "s_cmp_lt_u32 s56, 0x40000\n" \
  "s_cbranch_scc1 Hw2%=\n" \
  "Hw2%=d:\n" \
  "v_add_u32_sdwa v112, v26, v108 dst_sel:DWORD dst_unused:UNUSED_PAD src0_sel:DWORD src1_sel:WORD_0\n" \
  "v_add_u32_sdwa v113, v27, v108 dst_sel:DWORD dst_unused:UNUSED_PAD src0_sel:DWORD src1_sel:WORD_1\n" \
  "s_add_u32 m0, s61, 0\n" \
  "ds_read_b32 v108, v25 offset:64\n" \
  "buffer_load_dwordx4 v112, s[68:71], 0 offen nt lds\n" \
  "s_add_u32 m0, s61, 1024\n" \
  "s_nop 0\n" \
  "buffer_load_dwordx4 v113, s[68:71], 0 offen nt lds\n" \
  "s_waitcnt vmcnt(10)\n" \
  "s_mov_b64 exec, 1\n" \
  "ds_add_u32 v120, v122 offset:0\n" \
  "s_mov_b64 exec, -1\n" \
  "v_add_u32_sdwa v112, v26, v109 dst_sel:DWORD dst_unused:UNUSED_PAD src0_sel:DWORD src1_sel:WORD_0\n" \
  "v_add_u32_sdwa v113, v27, v109 dst_sel:DWORD dst_unused:UNUSED_PAD src0_sel:DWORD src1_sel:WORD_1\n" \
  "s_add_u32 m0, s61, 8704\n" \
  "ds_read_b32 v109, v25 offset:512\n" \
  "buffer_load_dwordx4 v112, s[72:75], 0 offen nt lds\n" \
  "s_add_u32 m0, s61, 9728\n" \
  "s_nop 0\n" \
  "buffer_load_dwordx4 v113, s[72:75], 0 offen nt lds\n" \
  "Hmid%=:\n" \
  "s_waitcnt vmcnt(10)\n" \
  "s_mov_b64 exec, 1\n" \
  "ds_add_u32 v120, v122 offset:4\n" \
  "s_mov_b64 exec, -1\n" \
  "v_add_u32_sdwa v112, v26, v110 dst_sel:DWORD dst_unused:UNUSED_PAD src0_sel:DWORD src1_sel:WORD_0\n" \
  "v_add_u32_sdwa v113, v27, v110 dst_sel:DWORD dst_unused:UNUSED_PAD src0_sel:DWORD src1_sel:WORD_1\n" \
  "s_add_u32 m0, s61, 17408\n" \
  "ds_read_b32 v110, v25 offset:960\n" \
  "buffer_load_dwordx4 v112, s[76:79], 0 offen nt lds\n" \
  "s_add_u32 m0, s61, 18432\n" \
  "s_nop 0\n" \
  "buffer_load_dwordx4 v113, s[76:79], 0 offen nt lds\n" \
  "s_waitcnt vmcnt(10)\n" \
  "s_mov_b64 exec, 1\n" \
  "ds_add_u32 v120, v122 offset:8\n" \
  "s_mov_b64 exec, -1\n" \
  "v_add_u32_sdwa v112, v26, v111 dst_sel:DWORD dst_unused:UNUSED_PAD src0_sel:DWORD src1_sel:WORD_0\n" \
  "v_add_u32_sdwa v113, v27, v111 dst_sel:DWORD dst_unused:UNUSED_PAD src0_sel:DWORD src1_sel:WORD_1\n" \
  "s_add_u32 m0, s61, 26112\n" \
  "ds_read_b32 v111, v25 offset:1408\n" \
  "buffer_load_dwordx4 v112, s[80:83], 0 offen nt lds\n" \
  "s_add_u32 m0, s61, 27136\n" \
  "s_nop 0\n" \
  "buffer_load_dwordx4 v113, s[80:83], 0 offen nt lds\n" \
  "s_waitcnt vmcnt(10)\n" \
  "s_mov_b64 exec, 1\n" \
  "ds_add_u32 v120, v122 offset:12\n" \
  "s_mov_b64 exec, -1\n" \
  "Hend%=:\n" \
  "v_add_u32 v120, 16, v120\n" \
  "s_add_u32 s53, s53, 1\n" \
  "s_branch Hloop%=\n" \
  "Hfin%=:\n" \
  "ds_read_b128 v[128:131], v28 offset:0\n" \
  "ds_read_b64_tr_b16 v[132:133], v29 offset:0\n" \
  "ds_read_b64_tr_b16 v[134:135], v30 offset:0\n" \
  "ds_read_b64_tr_b16 v[136:137], v31 offset:0\n" \
  "ds_read_b64_tr_b16 v[138:139], v32 offset:0\n" \
  "ds_read_b128 v[140:143], v28 offset:8704\n" \
  "ds_read_b64_tr_b16 v[144:145], v29 offset:8704\n" \
  "ds_read_b64_tr_b16 v[146:147], v30 offset:8704\n" \
  "ds_read_b64_tr_b16 v[148:149], v31 offset:8704\n" \
  "ds_read_b64_tr_b16 v[150:151], v32 offset:8704\n" \
  "ds_read_b128 v[152:155], v28 offset:17408\n" \
  "ds_read_b64_tr_b16 v[156:157], v29 offset:17408\n" \
  "ds_read_b64_tr_b16 v[158:159], v30 offset:17408\n" \
  "ds_read_b64_tr_b16 v[160:161], v31 offset:17408\n" \
  "ds_read_b64_tr_b16 v[162:163], v32 offset:17408\n" \
  "ds_read_b128 v[164:167], v28 offset:26112\n" \
  "ds_read_b64_tr_b16 v[168:169], v29 offset:26112\n" \
  "ds_read_b64_tr_b16 v[170:171], v30 offset:26112\n" \
  "ds_read_b64_tr_b16 v[172:173], v31 offset:26112\n" \
  "ds_read_b64_tr_b16 v[174:175], v32 offset:26112\n" \
  "s_mov_b64 exec, 1\n" \
  "ds_add_u32 v126, v122 offset:320\n" \
  "s_mov_b64 exec, -1\n" \
  "s_add_u32 s54, s54, 1\n" \
  "s_and_b32 s57, s54, 3\n" \
  "s_mov_b32 s60, 34816\n" \
  "s_cmp_eq_u32 s57, 0\n" \
  "s_cselect_b32 s60, 0xfffe6800, s60\n" \
  "v_add_u32 v28, s60, v28\n" \
  "v_add_u32 v29, s60, v29\n" \
  "v_add_u32 v30, s60, v30\n" \
  "v_add_u32 v31, s60, v31\n" \
  "v_add_u32 v32, s60, v32\n" \
  "v_add_u32 v121, 16, v121\n" \
  "v_add_u32 v126, 4, v126\n" \
  "s_waitcnt lgkmcnt(0)\n" \
  "v_mfma_f32_16x16x32_bf16 v[64:67], v[128:131], v[132:135], v[64:67]\n" \
  "v_mfma_f32_16x16x32_bf16 v[68:71], v[128:131], v[136:139], v[68:71]\n" \
  "v_mfma_f32_16x16x32_bf16 v[72:75], v[140:143], v[144:147], v[72:75]\n" \
  "v_mfma_f32_16x16x32_bf16 v[76:79], v[140:143], v[148:151], v[76:79]\n" \
  "v_mfma_f32_16x16x32_bf16 v[80:83], v[152:155], v[156:159], v[80:83]\n" \
  "v_mfma_f32_16x16x32_bf16 v[84:87], v[152:155], v[160:163], v[84:87]\n" \
  "v_mfma_f32_16x16x32_bf16 v[88:91], v[164:167], v[168:171], v[88:91]\n" \
  "v_mfma_f32_16x16x32_bf16 v[92:95], v[164:167], v[172:175], v[92:95]\n" \
  "s_cmp_eq_u32 s55, 0\n" \
  "s_cbranch_scc1 Hlast%=\n" \
  "Hrec%=:\n" \
  "s_add_u32 s57, s54, 4\n" \
  "s_cmp_gt_u32 s57, s53\n" \
  "s_cbranch_scc1 Hrec2%=\n" \
  "s_mov_b32 s56, 0\n" \
  "Hr1%=:\n" \
  "ds_read_b32 v124, v121 offset:204\n" \
  "s_add_u32 s56, s56, 1\n" \
  "s_waitcnt lgkmcnt(0)\n" \
  "v_readfirstlane_b32 s57, v124\n" \
  "s_cmp_ge_u32 s57, 4\n" \
  "s_cbranch_scc1 Hr1%=d\n" \
  "s_sleep 1\n" \
  "s_cmp_lt_u32 s56, 0x40000\n" \
  "s_cbranch_scc1 Hr1%=\n" \
  "Hr1%=d:\n" \
  "s_mov_b32 s55, 1\n" \
  "s_branch Hfin%=\n" \
  "Hrec2%=:\n" \
  "s_lshl_b32 s57, s53, 2\n" \
  "s_add_u32 s57, s57, 146480\n" \
  "s_branch Hgo%=\n" \
  "Hlad%=:\n" \
  "s_waitcnt vmcnt(8)\n" \
  "s_mov_b64 exec, 1\n" \
  "ds_add_u32 v120, v122 offset:0\n" \
  "s_mov_b64 exec, -1\n" \
  "s_waitcnt vmcnt(6)\n" \
  "s_mov_b64 exec, 1\n" \
  "ds_add_u32 v120, v122 offset:4\n" \
  "s_mov_b64 exec, -1\n" \
  "s_waitcnt vmcnt(4)\n" \
  "s_mov_b64 exec, 1\n" \
  "ds_add_u32 v120, v122 offset:8\n" \
  "s_mov_b64 exec, -1\n" \
  "s_waitcnt vmcnt(2)\n" \
  "s_mov_b64 exec, 1\n" \
  "ds_add_u32 v120, v122 offset:12\n" \
  "s_mov_b64 exec, -1\n" \
  "s_waitcnt vmcnt(0)\n" \
  "s_mov_b64 exec, 1\n" \
  "ds_add_u32 v120, v122 offset:16\n" \
  "s_mov_b64 exec, -1\n" \
  "s_mov_b32 s55, 0\n" \
  "s_setprio 0\n" \
  "Hlast%=:\n" \
  "s_add_u32 s57, s54, 1\n" \
  "s_cmp_ge_u32 s57, s52\n" \
  "s_cbranch_scc1 Hl2%=\n" \
  "s_mov_b32 s56, 0\n" \
  "Hl1%=:\n" \
  "ds_read_b32 v124, v121 offset:204\n" \
  "s_add_u32 s56, s56, 1\n" \
  "s_waitcnt lgkmcnt(0)\n" \
  "v_readfirstlane_b32 s57, v124\n" \
  "s_cmp_ge_u32 s57, 4\n" \
  "s_cbranch_scc1 Hl1%=d\n" \
  "s_sleep 2\n" \
  "s_cmp_lt_u32 s56, 0x40000\n" \
  "s_cbranch_scc1 Hl1%=\n" \
  "Hl1%=d:\n" \
  "s_branch Hfin%=\n" \
  "Hl2%=:\n" \
  "s_setprio 3\n" \
  "s_mov_b32 s56, 0\n" \
  "Hl2r%=:\n" \
  "ds_read_b32 v124, v121 offset:44\n" \
  "s_add_u32 s56, s56, 1\n" \
  "s_waitcnt lgkmcnt(0)\n" \
  "v_readfirstlane_b32 s57, v124\n" \
  "s_cmp_ge_u32 s57, 4\n" \
  "s_cbranch_scc1 Hl2r%=d\n" \
  "s_sleep 1\n" \
  "s_cmp_lt_u32 s56, 0x40000\n" \
  "s_cbranch_scc1 Hl2r%=\n" \
  "Hl2r%=d:\n" \
  "ds_read_b64_tr_b16 v[132:133], v29 offset:0\n" \
  "ds_read_b64_tr_b16 v[134:135], v30 offset:0\n" \
  "ds_read_b64_tr_b16 v[136:137], v31 offset:0\n" \
  "ds_read_b64_tr_b16 v[138:139], v32 offset:0\n" \
  "ds_read_b64_tr_b16 v[144:145], v29 offset:8704\n" \
  "ds_read_b64_tr_b16 v[146:147], v30 offset:8704\n" \
  "ds_read_b64_tr_b16 v[148:149], v31 offset:8704\n" \
  "ds_read_b64_tr_b16 v[150:151], v32 offset:8704\n" \
  "ds_read_b64_tr_b16 v[156:157], v29 offset:17408\n" \
  "ds_read_b64_tr_b16 v[158:159], v30 offset:17408\n" \
  "ds_read_b64_tr_b16 v[160:161], v31 offset:17408\n" \
  "ds_read_b64_tr_b16 v[162:163], v32 offset:17408\n" \
  "ds_read_b64_tr_b16 v[168:169], v29 offset:26112\n" \
  "ds_read_b64_tr_b16 v[170:171], v30 offset:26112\n" \
  "ds_read_b64_tr_b16 v[172:173], v31 offset:26112\n" \
  "ds_read_b64_tr_b16 v[174:175], v32 offset:26112\n" \
  "s_mov_b32 s56, 0\n" \
  "Hl3%=:\n" \
  "ds_read_b32 v124, v121 offset:196\n" \
  "ds_read_b128 v[128:131], v28 offset:0\n" \
  "ds_read_b128 v[140:143], v28 offset:8704\n" \
  "s_add_u32 s56, s56, 1\n" \
  "s_waitcnt lgkmcnt(0)\n" \
  "v_readfirstlane_b32 s57, v124\n" \
  "s_cmp_ge_u32 s57, 4\n" \
  "s_cbranch_scc1 Hl3%=d\n" \
  "s_sleep 1\n" \
  "s_cmp_lt_u32 s56, 0x40000\n" \
  "s_cbranch_scc1 Hl3%=\n" \
  "Hl3%=d:\n" \
  "v_mfma_f32_16x16x32_bf16 v[64:67], v[128:131], v[132:135], v[64:67]\n" \
  "v_mfma_f32_16x16x32_bf16 v[68:71], v[128:131], v[136:139], v[68:71]\n" \
  "v_mfma_f32_16x16x32_bf16 v[72:75], v[140:143], v[144:147], v[72:75]\n" \
  "v_mfma_f32_16x16x32_bf16 v[76:79], v[140:143], v[148:151], v[76:79]\n" \
  "s_nop 4\n" \
  "v_add_f32 v64, v64, v65\n" \
  "v_add_f32 v66, v66, v67\n" \
  "v_add_f32 v68, v68, v69\n" \
  "v_add_f32 v70, v70, v71\n" \
  "v_add_f32 v72, v72, v73\n" \
  "v_add_f32 v74, v74, v75\n" \
  "v_add_f32 v76, v76, v77\n" \
  "v_add_f32 v78, v78, v79\n" \
  "v_add_f32 v64, v64, v66\n" \
  "v_add_f32 v68, v68, v70\n" \
  "v_add_f32 v72, v72, v74\n" \
  "v_add_f32 v76, v76, v78\n" \
  "s_nop 0\n" \
  "v_permlane16_swap_b32 v64, v68\n" \
  "v_permlane16_swap_b32 v72, v76\n" \
  "s_nop 0\n" \
  "v_add_f32 v64, v64, v68\n" \
  "s_mov_b32 exec_lo, 0\n" \
  "v_add_f32 v64, v72, v76\n" \
  "s_mov_b32 exec_lo, -1\n" \
  "v_mul_f32 v64, 0x30800000, v64\n" \
  "v_mul_f32 v64, s64, v64\n" \
  "v_cvt_pk_bf16_f32 v64, v64, v64\n" \
  "global_store_short v33, v64, %[outp] offset:0\n" \
  "s_mov_b32 s56, 0\n" \
  "Hl5%=:\n" \
  "ds_read_b32 v124, v121 offset:204\n" \
  "ds_read_b128 v[152:155], v28 offset:17408\n" \
  "ds_read_b128 v[164:167], v28 offset:26112\n" \
  "s_add_u32 s56, s56, 1\n" \
  "s_waitcnt lgkmcnt(0)\n" \
  "v_readfirstlane_b32 s57, v124\n" \
  "s_cmp_ge_u32 s57, 4\n" \
  "s_cbranch_scc1 Hl5%=d\n" \
  "s_cmp_lt_u32 s56, 0x40000\n" \
  "s_cbranch_scc1 Hl5%=\n" \
  "Hl5%=d:\n" \
  "v_mfma_f32_16x16x32_bf16 v[80:83], v[152:155], v[156:159], v[80:83]\n" \
  "v_mfma_f32_16x16x32_bf16 v[84:87], v[152:155], v[160:163], v[84:87]\n" \
  "v_mfma_f32_16x16x32_bf16 v[88:91], v[164:167], v[168:171], v[88:91]\n" \
  "v_mfma_f32_16x16x32_bf16 v[92:95], v[164:167], v[172:175], v[92:95]\n" \
  "s_nop 4\n" \
  "v_add_f32 v80, v80, v81\n" \
  "v_add_f32 v82, v82, v83\n" \
  "v_add_f32 v84, v84, v85\n" \
  "v_add_f32 v86, v86, v87\n" \
  "v_add_f32 v88, v88, v89\n" \
  "v_add_f32 v90, v90, v91\n" \
  "v_add_f32 v92, v92, v93\n" \
  "v_add_f32 v94, v94, v95\n" \
  "v_add_f32 v80, v80, v82\n" \
  "v_add_f32 v84, v84, v86\n" \
  "v_add_f32 v88, v88, v90\n" \
  "v_add_f32 v92, v92, v94\n" \
  "s_nop 0\n" \
  "v_permlane16_swap_b32 v80, v84\n" \
  "v_permlane16_swap_b32 v88, v92\n" \
  "s_nop 0\n" \
  "v_add_f32 v80, v80, v84\n" \
  "s_mov_b32 exec_lo, 0\n" \
  "v_add_f32 v80, v88, v92\n" \
  "s_mov_b32 exec_lo, -1\n" \
  "s_branch Ho2%=\n" \
  "Hout%=:\n" \
  "s_mov_b32 s64, 0\n" \
  "global_store_short v33, v64, %[outp]\n" \
  "Ho2%=:\n" \
  "v_mul_f32 v80, 0x30800000, v80\n" \
  "v_mul_f32 v80, s64, v80\n" \
  "v_cvt_pk_bf16_f32 v80, v80, v80\n" \
  "global_store_short v33, v80, %[outp] offset:512\n" \
  "din_end:\n" \
  "s_waitcnt lgkmcnt(0)\n"
#define HELPER_CLOB \
  "v2", "v3", "v4", "v5", "v6", "v7", "v8", "v9", "v10", "v11", "v12", "v13", "v14", "v15", "v64", "v65", "v66", "v67", "v68", "v69", "v70", "v71", "v72", "v73", \
  "v74", "v75", "v76", "v77", "v78", "v79", "v80", "v81", "v82", "v83", "v84", "v85", "v86", "v87", "v88", "v89", "v90", "v91", "v92", "v93", "v94", "v95", "v96", "v97", \
  "v98", "v99", "v100", "v101", "v102", "v103", "v104", "v105", "v106", "v107", "v108", "v109", "v110", "v111", "v112", "v113", "v114", "v115", "v116", "v117", "v118", "v119", "v120", "v121", \
  "v122", "v123", "v124", "v125", "v126", "v127", "v128", "v129", "v130", "v131", "v132", "v133", "v134", "v135", "v136", "v137", "v138", "v139", "v140", "v141", "v142", "v143", "v144", "v145", \
  "v146", "v147", "v148", "v149", "v150", "v151", "v152", "v153", "v154", "v155", "v156", "v157", "v158", "v159", "v160", "v161", "v162", "v163", "v164", "v165", "v166", "v167", "v168", "v169", \
  "v170", "v171", "v172", "v173", "v174", "v175", "v176", "v177", "v178", "v179", "v180", "v181", "v182", "v183", "s40", "s41", "s42", "s43", "s44", "s45", "s46", "s47", "s48", "s49", \
  "s50", "s51", "s52", "s53", "s54", "s55", "s56", "s57", "s58", "s59", "s60", "s61", "s62", "s63", "s64", "s65", "s66", "s67", "s68", "s69", "s70", "s71", "s72", "s73", \
  "s74", "s75", "s76", "s77", "s78", "s79", "s80", "s81", "s82", "s83", "vcc", "scc", "memory"

__device__ __forceinline__ u32 lane_id() {
  u32 r;
  asm volatile("v_mbcnt_lo_u32_b32 %0, -1, 0\n\tv_mbcnt_hi_u32_b32 %0, -1, %0" : "=v"(r));
  return r;
}
__device__ __forceinline__ u32 swz(u32 row, u32 ch) { return 256u * row + 16u * (ch ^ (((row & 3) << 2) | ((row >> 2) & 3))); }

__global__ __launch_bounds__(512, 2) void din_kernel(const u16* __restrict__ tgt, const u16* __restrict__ hist,
                                                     const u16* __restrict__ mask, const u16* __restrict__ w1,
                                                     const u16* __restrict__ b1, const u16* __restrict__ w2,
                                                     const u16* __restrict__ b2, u16* __restrict__ out
) {
  __shared__ __attribute__((aligned(1024))) char lds_raw[LDS_BYTES];
  asm volatile("" ::"v"((__attribute__((address_space(3))) char*)lds_raw));
  const u32 wave = __builtin_amdgcn_readfirstlane(threadIdx.x >> 6);
  const u32 b0 = blockIdx.x * 4;
  u32 lane0, wv;
  asm volatile(
      "v_mbcnt_lo_u32_b32 v247, -1, 0\n\t"
      "v_mbcnt_hi_u32_b32 v247, -1, v247\n\t"
      "s_cmp_lg_u32 %1, 1\n\t"
      "s_cbranch_scc1 din_pf1\n\t"
      "s_getpc_b64 s[40:41]\n"
      "din_pf:\n\t"
      "s_bfe_u32 s42, %2, 0x50003\n\t"
      "s_lshl_b32 s42, s42, 9\n\t"
      "s_min_u32 s42, s42, din_end-din_pf-512\n\t"
      "s_add_u32 s40, s40, s42\n\t"
      "s_addc_u32 s41, s41, 0\n\t"
      "v_lshlrev_b32 v250, 7, v247\n\t"
      "s_mov_b64 exec, 15\n\t"
      "global_load_dword v174, v250, s[40:41]\n\t"
      "s_mov_b64 exec, -1\n"
      "din_pf1:"
      : "={v247}"(lane0) : "s"(wave), "s"(blockIdx.x) : "s40", "s41", "s42", "scc", "v174", "v250", "memory");
  const u32 w1lo = (u32)(size_t)w1, w1hi = (u32)((size_t)w1 >> 32);
  asm volatile("" ::"s"(tgt), "s"(hist), "s"(mask), "s"(w1), "s"(b1), "s"(w2), "s"(b2), "s"(out));
  asm volatile(ENTRY_ASM
               : "={v177}"(wv), "+{v247}"(lane0)
               : [wave] "s"(wave), [blk] "s"(blockIdx.x), [mask] "s"(mask), [w2] "s"(w2), [tlo] "s"((u32)(size_t)tgt),
                 [thi] "s"((u32)((size_t)tgt >> 32)), [w1lo] "s"(w1lo), [w1hi] "s"(w1hi)
               : "s40", "s41", "s42", "s43", "s44", "scc", "v175", "v240", "v241", "v242", "v243", "v244", "v245", "v246",
                 "v248", "v249", "v250", "memory");
  if (wave >= 4) {
    const u32 q = wave - 4;
    const u32 lane = lane_id(), lg = lane >> 4, l15 = lane & 15, tq = l15 >> 2, tp = lane & 3;
    u32x16 K;
    u32x8 K2;
    u32x4 K3;
#pragma unroll
    for (int i = 0; i < 4; ++i) K[i] = lane * 1024 + 256 * i;
    K[4] = (12 + q) * SLOTB + 64 * (8 * lg + tq) + 16 * ((tp >> 1) ^ ((2 * lg) & 3)) + 8 * (tp & 1);
    K[5] = K[4] ^ 32;
    K[6] = (K[4] ^ 16) + 256;
    K[7] = K[6] ^ 32;
    K[8] = lane * 16;
    K[9] = OFFS + (8 * lg + 2 * q) * 2;
    K[10] = 16 * (l15 ^ ((lg << 2) | ((2 * q) & 3)));
    K[11] = 16 * (l15 ^ ((lg << 2) | ((2 * q + 1) & 3)));
    K[12] = 8192 + (lane & 7) * 64 + lg * 16;
    K[13] = swz(8 * lg + tq, 4 * q + (tp >> 1)) + 8 * (tp & 1);
    K[14] = swz(8 * lg + 4 + tq, 4 * q + (tp >> 1)) + 8 * (tp & 1);
    K[15] = swz(8 * lg + tq, 4 * q + 2 + (tp >> 1)) + 8 * (tp & 1);
    K2[0] = swz(8 * lg + 4 + tq, 4 * q + 2 + (tp >> 1)) + 8 * (tp & 1);
    K2[1] = ((b0 + (lane >> 5)) * D + 32 * q + (lane & 31)) * 2;
    K2[2] = OFFS + q * 448;
    K2[3] = MSKF + q * 4;
    K2[4] = (lane >> 2) * 256 + 16 * ((lane & 3) ^ lg);
    K2[5] = TR + (lane & 3) * 256 + lg * 16;
    K2[6] = CB + q * 1024 + lane * 16;
    K2[7] = 0;
    K3[0] = BW + 256 + lane * 4;
    K3[1] = BW + (32 * q + (lane & 31)) * 2;
    K3[2] = KT + q * 288 + (lane & 31) * 4;
    K3[3] = KT + q * 288 + 256 + (lane & 4) * 4;
    const size_t hb = (size_t)hist + (size_t)b0 * (L * D * 2);
    asm volatile(HELPER_ASM
                 : "+{v[16:31]}"(K), "+{v[32:39]}"(K2), "+{v[40:43]}"(K3)
                 : [q] "s"(q), [hlo] "s"((u32)hb), [hhi] "s"((u32)(hb >> 32)), [outp] "s"(out), [b1p] "s"(b1), [b2p] "s"(b2),
                   [w1lo] "s"(w1lo), [w1hi] "s"(w1hi)
                 : HELPER_CLOB);
  } else {
    const u32 w = wave;
    const u32 lane = lane_id(), lg = lane >> 4, l15 = lane & 15, c = lane & 31, hf = lane >> 5, tq = l15 >> 2, tp = lane & 3;
    u32x16 K, A;
    u32x8 I;
    const u32 one = (c & 1) ? 0xffff0000u : 0x0000ffffu, dw = (c & 7) >> 1;
    const bool h0 = (c >> 3) == hf, h1 = (c >> 3) == 2 + hf;
#pragma unroll
    for (int i = 0; i < 4; ++i) { I[i] = (h0 && dw == i) ? one : 0u; I[4 + i] = (h1 && dw == i) ? one : 0u; }
    const u32 ws = (4 + w) * SLOTB;
    const u32 prow = (c & 0x13) | ((c & 4) << 1) | ((c & 8) >> 1);
    K[0] = ws + 64 * prow + 16 * (hf ^ ((prow >> 2) & 3));
    K[1] = K[0] ^ 32;
    K[2] = ws + 64 * (8 * lg + tq) + 16 * ((tp >> 1) ^ ((2 * lg) & 3)) + 8 * (tp & 1);
    K[3] = K[2] ^ 32;
    K[4] = (K[2] ^ 16) + 256;
    K[5] = K[4] ^ 32;
    K[6] = TR + (lane & 3) * 256 + lg * 16;
    K[7] = TR + prow * 2;
    K[8] = CB + w * 1024 + lane * 16;
    K[9] = KT + w * 288 + hf * 16;
    K[10] = CNT;
    K[11] = swz(c, hf);
    K[12] = 8192 + (2 * w + hf) * 64 + c * 2;
    K[13] = MSKF + (8 * (c & 3) + (c >> 2)) * 16;
    K[14] = CNT + 192 - 16 + lane * 4;
    K[15] = CNT + 448 + 16 * w;
#pragma unroll
    for (int i = 0; i < 16; ++i) A[i] = 0;
    A[0] = BW + 256 + (32 * w + c) * 2;
    A[1] = wv;
    A[8] = A[11] = hf ? 0x3b2cu : 0xbfb9u;
    const u32 xdst = XW + w * 2048;
    asm volatile(COMPUTE_ASM
                 : "+{v[176:191]}"(A), "+{v[224:231]}"(I), "+{v[240:255]}"(K)
                 : [w1lo] "s"(w1lo), [w1hi] "s"(w1hi), [wdst] "s"(w * SLOTB), [xdst] "s"(xdst), [xrel] "s"(xdst - ws)
                 : COMPUTE_CLOB);
  }
}

extern "C" void kernel_launch(void* const* d_in, const int* in_sizes, int n_in, void* d_out, int out_size,
                              void* d_ws, size_t ws_size, hipStream_t stream) {
  const int B = in_sizes[0] / D;
  din_kernel<<<B / 4, 512, 0, stream>>>((const u16*)d_in[0], (const u16*)d_in[1], (const u16*)d_in[2], (const u16*)d_in[3],
                                        (const u16*)d_in[4], (const u16*)d_in[5], (const u16*)d_in[6], (u16*)d_out
  );
}
```
